```python
import torch

def _softmax(x, axis=-1):
    return torch.softmax(x, dim=axis)
def _gelu(x):
    return torch.nn.functional.gelu(x, approximate="tanh")
def _silu(x):
    return torch.nn.functional.silu(x)
def _sigmoid(x):
    return torch.sigmoid(x)
def _layernorm(x, axis=-1, eps=1e-5):
    m = x.mean(dim=axis, keepdim=True)
    v = x.var(dim=axis, keepdim=True, unbiased=False)
    return (x - m) / torch.sqrt(v + eps)
def _scatter_add(tgt, idx, src, axis=0):
    return tgt.clone().index_add_(axis, idx, src)
def _segment_sum(src, seg, n):
    out = torch.zeros((n,) + src.shape[1:], dtype=src.dtype)
    return out.index_add_(0, seg, src)
def _pad(x, pw):
    flat = tuple(v for pair in reversed(pw) for v in pair)
    return torch.nn.functional.pad(x, flat)

kernel_name = 'parallel_sum__ssm_conv_s4__glu_gated_bilinear__prompt'

def setup_inputs():

    _w = (lambda a: (lambda t: t.to(torch.bfloat16) if t.is_floating_point() else t)(torch.as_tensor(a)))
    def _A_setup():
        B, L, D, N = 1, 2048, 448, 256
        A_re = -(lambda _x: torch.exp(torch.as_tensor(_x)))((lambda *s: torch.randn(*s, dtype=torch.bfloat16) * 6.4)(D, N))
        A_im = torch.pi * (lambda *s: torch.randn(*s, dtype=torch.bfloat16) * 0.05)(D, N)
        B_re = (lambda *s: torch.randn(*s, dtype=torch.bfloat16) * 0.1)(D, N)
        B_im = (lambda *s: torch.randn(*s, dtype=torch.bfloat16) * 0.1)(D, N)
        C_re = (lambda *s: torch.randn(*s, dtype=torch.bfloat16) * 0.1)(D, N)
        C_im = (lambda *s: torch.randn(*s, dtype=torch.bfloat16) * 0.1)(D, N)
        log_dt = (lambda *s: torch.randn(*s, dtype=torch.bfloat16) * 0.05)(D) * 0.5 - 4.0
        Dskip = (lambda *s: torch.randn(*s, dtype=torch.bfloat16) * 0.05)(D)
        return {
            "u": _w((lambda *s: torch.randn(*s, dtype=torch.bfloat16) * 0.05)(B, L, D)),
            "A_re": _w(A_re),
            "A_im": _w(A_im),
            "B_re": _w(B_re),
            "B_im": _w(B_im),
            "C_re": _w(C_re),
            "C_im": _w(C_im),
            "log_dt": _w(log_dt),
            "Dskip": _w(Dskip),
        }
    _A_in = _A_setup()

    def _B_setup():
        B, S, D, F = 1, 2048, 448, 1792
        return {
            "x": _w((lambda *s: torch.randn(*s, dtype=torch.bfloat16) * 0.05)(B, S, D)),
            "w_gate": _w((lambda *s: torch.randn(*s, dtype=torch.bfloat16) * 0.4)(D, F) * (D ** -0.5)),
            "w_up": _w((lambda *s: torch.randn(*s, dtype=torch.bfloat16) * 0.4)(D, F) * (D ** -0.5)),
            "w_down": _w((lambda *s: torch.randn(*s, dtype=torch.bfloat16) * 0.4)(F, D) * (F ** -0.5)),
        }
    _B_in = _B_setup()

    return {
        "A_u": _A_in["u"],
        "A_A_re": _A_in["A_re"],
        "A_A_im": _A_in["A_im"],
        "A_B_re": _A_in["B_re"],
        "A_B_im": _A_in["B_im"],
        "A_C_re": _A_in["C_re"],
        "A_C_im": _A_in["C_im"],
        "A_log_dt": _A_in["log_dt"],
        "A_Dskip": _A_in["Dskip"],
        "B_x": _B_in["x"],
        "B_w_gate": _B_in["w_gate"],
        "B_w_up": _B_in["w_up"],
        "B_w_down": _B_in["w_down"],
    }

def reference(A_u, A_A_re, A_A_im, A_B_re, A_B_im, A_C_re, A_C_im, A_log_dt, A_Dskip, B_x, B_w_gate, B_w_up, B_w_down):
    def _A_ref(u, A_re, A_im, B_re, B_im, C_re, C_im, log_dt, Dskip):
        Bsz, L, D = u.shape
        dt = torch.exp(log_dt)[:, None]
        A = A_re + 1j * A_im
        Bc = B_re + 1j * B_im
        Cc = C_re + 1j * C_im
        logdA = dt * A
        dB = dt * Bc
        l = (lambda x, dt: x.to(dt))(torch.arange(L), A_re.dtype)
        P = torch.exp(logdA[:, :, None] * l[None, None, :])
        K = torch.einsum("dn,dnl,dn->dl", Cc, P, dB).real
        L2 = 2 * L
        Kp = _pad(K, ((0, 0), (0, L)))
        up = (lambda x, *d: x.permute(*d))(u, 0, 2, 1)
        up = _pad(up, ((0, 0), (0, 0), (0, L)))
        Kf = (lambda x, axes=(-2,-1): torch.fft.rfft2(x.float(), dim=axes))(Kp[:, None, :], axes=(-2, -1))
        Uf = (lambda x, axes=(-2,-1): torch.fft.rfft2(x.float(), dim=axes))(up[:, :, None, :], axes=(-2, -1))
        Yf = Uf * Kf[None, :, :, :]
        y = (lambda x, s, axes=(-2,-1): torch.fft.irfft2(x, s=s, dim=axes).to(torch.bfloat16))(Yf, (1, L2), axes=(-2, -1))[:, :, 0, :L]
        y = (lambda x, *d: x.permute(*d))(y, 0, 2, 1) + u * Dskip[None, None, :]
        return y

    def _B_ref(x, w_gate, w_up, w_down):
        g = torch.einsum("bsd,df->bsf", x, w_gate)
        u = torch.einsum("bsd,df->bsf", x, w_up)
        h = g * u
        return torch.einsum("bsf,fd->bsd", h, w_down)

    x = A_u
    xn = _layernorm(x, axis=-1)
    return x + _A_ref(xn, A_A_re, A_A_im, A_B_re, A_B_im, A_C_re, A_C_im, A_log_dt, A_Dskip) + _B_ref(xn, B_w_gate, B_w_up, B_w_down)
```

```cpp
#include <hip/hip_runtime.h>
#include <hip/hip_bf16.h>
typedef __hip_bfloat16 bf16;
#define L 2048
#define D 448
#define N 256
#define F 1792
__device__ __forceinline__ float bf2f(bf16 x){return __bfloat162float(x);}
__device__ __forceinline__ float rbf(float x){return __bfloat162float(__float2bfloat16(x));}

__global__ void ln_k(const bf16* x, bf16* xn){
  int t = blockIdx.x; int lane = threadIdx.x;
  float v[7]; float s=0;
  for(int i=0;i<7;i++){v[i]=bf2f(x[t*D+lane+64*i]); s+=v[i];}
  for(int o=32;o;o>>=1) s+=__shfl_xor(s,o);
  float m=s/D; float q=0;
  for(int i=0;i<7;i++){float d=v[i]-m;q+=d*d;}
  for(int o=32;o;o>>=1) q+=__shfl_xor(q,o);
  float var=rbf(q/D); float mb=rbf(m);
  float den=rbf(sqrtf(rbf(var+1e-5f)));
  for(int i=0;i<7;i++){ xn[t*D+lane+64*i]=__float2bfloat16(rbf(v[i]-mb)/den);}
}
__global__ void kgen_k(const bf16* Are,const bf16* Aim,const bf16* Bre,const bf16* Bim,const bf16* Cre,const bf16* Cim,const bf16* logdt,float* K){
  int d=blockIdx.y; int l=blockIdx.x*256+threadIdx.x;
  float dt=rbf(expf(bf2f(logdt[d])));
  float lf=rbf((float)l);
  float acc=0;
  for(int n=0;n<N;n++){
    float re=dt*bf2f(Are[d*N+n]), im=dt*bf2f(Aim[d*N+n]);
    float br=dt*bf2f(Bre[d*N+n]), bi=dt*bf2f(Bim[d*N+n]);
    float cr=bf2f(Cre[d*N+n]), ci=bf2f(Cim[d*N+n]);
    float e=expf(re*lf); float s,c; sincosf(im*lf,&s,&c);
    float pr=e*c, pi=e*s;
    float tr=cr*pr-ci*pi, ti=cr*pi+ci*pr;
    acc+=tr*br-ti*bi;
  }
  K[d*L+l]=acc;
}
__global__ void conv_k(const bf16* xn,const float* K,const bf16* Dskip,bf16* yA){
  int d=blockIdx.y; int t=blockIdx.x*256+threadIdx.x;
  float acc=0;
  for(int s=0;s<=t;s++) acc+=K[d*L+t-s]*bf2f(xn[s*D+d]);
  float y=rbf(acc);
  yA[t*D+d]=__float2bfloat16(y+rbf(bf2f(xn[t*D+d])*bf2f(Dskip[d])));
}
__global__ void g1_k(const bf16* xn,const bf16* wg,const bf16* wu,bf16* h){
  int f=blockIdx.x*256+threadIdx.x; int t=blockIdx.y;
  float g=0,u=0;
  for(int k=0;k<D;k++){float a=bf2f(xn[t*D+k]); g+=a*bf2f(wg[k*F+f]); u+=a*bf2f(wu[k*F+f]);}
  h[t*F+f]=__float2bfloat16(rbf(g)*rbf(u));
}
__global__ void g2_k(const bf16* h,const bf16* wd,const bf16* x,const bf16* yA,bf16* out){
  int d=blockIdx.x*64+threadIdx.x; int t=blockIdx.y;
  float a=0;
  for(int k=0;k<F;k++) a+=bf2f(h[t*F+k])*bf2f(wd[k*D+d]);
  out[t*D+d]=__float2bfloat16(rbf(bf2f(x[t*D+d])+bf2f(yA[t*D+d]))+rbf(a));
}
extern "C" void kernel_launch(void* const* d_in, const int* in_sizes, int n_in, void* d_out, int out_size, void* d_ws, size_t ws_size, hipStream_t stream){
  const bf16* x=(const bf16*)d_in[0];
  char* ws=(char*)d_ws;
  bf16* xn=(bf16*)ws; float* K=(float*)(ws+(4<<20)); bf16* yA=(bf16*)(ws+(8<<20)); bf16* h=(bf16*)(ws+(16<<20));
  ln_k<<<L,64,0,stream>>>(x,xn);
  kgen_k<<<dim3(8,D),256,0,stream>>>((const bf16*)d_in[1],(const bf16*)d_in[2],(const bf16*)d_in[3],(const bf16*)d_in[4],(const bf16*)d_in[5],(const bf16*)d_in[6],(const bf16*)d_in[7],K);
  conv_k<<<dim3(8,D),256,0,stream>>>(xn,K,(const bf16*)d_in[8],yA);
  g1_k<<<dim3(7,L),256,0,stream>>>(xn,(const bf16*)d_in[10],(const bf16*)d_in[11],h);
  g2_k<<<dim3(7,L),64,0,stream>>>(h,(const bf16*)d_in[12],x,yA,(bf16*)d_out);
}
```

```cpp
#include <hip/hip_runtime.h>
#include <hip/hip_bf16.h>
#include <stdint.h>

typedef __attribute__((ext_vector_type(8))) __bf16 bf16x8;
typedef __attribute__((ext_vector_type(4))) __bf16 bf16x4;
typedef __attribute__((ext_vector_type(2))) __bf16 bf16x2;
typedef __attribute__((ext_vector_type(8))) _Float16 f16x8;
typedef __attribute__((ext_vector_type(4))) _Float16 f16x4;
typedef __attribute__((ext_vector_type(2))) _Float16 f16x2;
typedef __attribute__((ext_vector_type(4))) float f32x4;
typedef __attribute__((ext_vector_type(4))) unsigned u32x4;
typedef __attribute__((ext_vector_type(2))) unsigned u32x2;
typedef __attribute__((ext_vector_type(4))) short s16x4;
typedef __attribute__((ext_vector_type(8))) short s16x8;
#define LAS __attribute__((address_space(3)))

#define SEQ 2048
#define DM 448
#define NS 256
#define FF 1792

#define WS_H 0
#define WS_WD (8u << 20)
#define WS_XT (10u << 20)
#define WS_YT (12u << 20)
#define WS_YB (14u << 20)

__device__ __forceinline__ unsigned pk_bf16(float a, float b) {
  bf16x2 v = {(__bf16)a, (__bf16)b};
  return __builtin_bit_cast(unsigned, v);
}
__device__ __forceinline__ unsigned pk_f16(float a, float b) {
  f16x2 v = {(_Float16)a, (_Float16)b};
  return __builtin_bit_cast(unsigned, v);
}
__device__ __forceinline__ float bf_lo(unsigned u) { return __uint_as_float(u << 16); }
__device__ __forceinline__ float bf_hi(unsigned u) { return __uint_as_float(u & 0xffff0000u); }
__device__ __forceinline__ float rbf(float x) { return (float)(__bf16)x; }
__device__ __forceinline__ float bf1(const void* p, int i) {
  return __uint_as_float(((unsigned)((const unsigned short*)p)[i]) << 16);
}
__device__ __forceinline__ u32x2 tr_read(const char* lds) {
  s16x4 v = __builtin_amdgcn_ds_read_tr16_b64_v4i16((LAS s16x4*)lds);
  return __builtin_bit_cast(u32x2, v);
}
#define WAIT_V(n) asm volatile("s_waitcnt vmcnt(" #n ")" ::: "memory")
#define WAIT_L(n) asm volatile("s_waitcnt lgkmcnt(" #n ")" ::: "memory")
#define BAR() asm volatile("s_waitcnt lgkmcnt(0)\n\ts_barrier" ::: "memory")
__device__ __forceinline__ void glds16(const char* sbase, unsigned voff, unsigned lds_dst) {
  unsigned keep;
  asm volatile("s_mov_b32 %0, m0\n\ts_mov_b32 m0, %3\n\ts_nop 0\n\tglobal_load_lds_dwordx4 %1, %2\n\ts_mov_b32 m0, %0"
               : "=&s"(keep) : "v"(voff), "s"(sbase), "s"(lds_dst) : "memory");
}

#define K1_BSLOT 28672
#define K1_ASLOT 16384
#define K1_LA (4 * K1_BSLOT)
#define K1_LWD (K1_LA + 2 * K1_ASLOT)
#define K1_LXT (K1_LWD + 7168)
#define K1_LDS (K1_LXT + 8192)

__global__ __launch_bounds__(512, 2) void k1_kernel(const char* __restrict__ x, const char* __restrict__ wg,
                                                    const char* __restrict__ wu, const char* __restrict__ wd,
                                                    char* __restrict__ ws) {
  extern __shared__ __attribute__((aligned(16))) char lds[];
  const int tid = threadIdx.x, lane = tid & 63;
  const int wid = __builtin_amdgcn_readfirstlane(tid >> 6);
  const int bid = blockIdx.x;
  const int xcd = bid & 7, idx = bid >> 3;
  const int mt = 8 * (xcd & 1) + (idx & 7);
  const int jt = 4 * (xcd >> 1) + (idx >> 3);
  const int r = lane & 15, q = lane >> 4;
  const int w4 = wid & 3;

  const char* wsrc = ((w4 >> 1) ? wu : wg) + jt * 224;
  unsigned goff[7];
#pragma unroll
  for (int i = 0; i < 7; ++i) {
    int sg = 64 * ((w4 & 1) * 7 + i) + lane;
    int rho = sg / 14, ch = sg - rho * 14;
    int krow = (rho & 32) + 8 * ((rho >> 2) & 3) + 4 * ((rho >> 4) & 1) + (rho & 3);
    goff[i] = krow * (FF * 2) + ch * 16;
  }
  const int ldst = (w4 >> 1) * 14336 + (w4 & 1) * 7168;
  const unsigned lbase = (unsigned)(size_t)(LAS char*)lds;
  auto dma_chunk = [&](int c) {
    const char* src = wsrc + (size_t)c * (64 * FF * 2);
    const unsigned dst = lbase + (c & 3) * K1_BSLOT + ldst;
#pragma unroll
    for (int i = 0; i < 7; ++i) glds16(src, goff[i], dst + i * 1024);
  };

  if (wid < 4) {
    dma_chunk(0); dma_chunk(1); dma_chunk(2); dma_chunk(3);
    f32x4 acc[14][2];
#pragma unroll
    for (int i = 0; i < 14; ++i) { acc[i][0] = (f32x4){0, 0, 0, 0}; acc[i][1] = (f32x4){0, 0, 0, 0}; }
    const int qq = (lane & 15) >> 2, p = lane & 3;
    const int boff = (4 * q + qq) * 224 + p * 8;
    const int aoff = K1_LA + (2 * wid) * 1024 + lane * 16;
#pragma unroll 1
    for (int c = 0; c < 7; ++c) {
      if (c == 0) WAIT_V(21); else if (c == 1) WAIT_V(14); else if (c == 2) WAIT_V(7); else WAIT_V(0);
      BAR();
      const char* Bp = lds + (c & 3) * K1_BSLOT + boff;
      const char* Ap = lds + aoff + (c & 1) * K1_ASLOT;
#pragma unroll
      for (int ks = 0; ks < 2; ++ks) {
        bf16x8 a0 = *(const bf16x8*)(Ap + ks * 8192);
        bf16x8 a1 = *(const bf16x8*)(Ap + ks * 8192 + 1024);
#pragma unroll
        for (int ct = 0; ct < 14; ++ct) {
          const int o = (ct / 7) * 14336 + (ct % 7) * 32 + ks * 32 * 224;
          u32x2 lo = tr_read(Bp + o), hi = tr_read(Bp + o + 16 * 224);
          u32x4 b4 = {lo[0], lo[1], hi[0], hi[1]};
          bf16x8 b = __builtin_bit_cast(bf16x8, b4);
          acc[ct][0] = __builtin_amdgcn_mfma_f32_16x16x32_bf16(b, a0, acc[ct][0], 0, 0, 0);
          acc[ct][1] = __builtin_amdgcn_mfma_f32_16x16x32_bf16(b, a1, acc[ct][1], 0, 0, 0);
        }
      }
    }
#pragma unroll
    for (int tt = 0; tt < 2; ++tt) {
      u32x2 hv[7];
#pragma unroll
      for (int ct = 0; ct < 7; ++ct) {
        unsigned g01 = pk_bf16(acc[ct][tt][0], acc[ct][tt][1]), g23 = pk_bf16(acc[ct][tt][2], acc[ct][tt][3]);
        unsigned u01 = pk_bf16(acc[ct + 7][tt][0], acc[ct + 7][tt][1]), u23 = pk_bf16(acc[ct + 7][tt][2], acc[ct + 7][tt][3]);
        hv[ct][0] = pk_bf16(bf_lo(g01) * bf_lo(u01), bf_hi(g01) * bf_hi(u01));
        hv[ct][1] = pk_bf16(bf_lo(g23) * bf_lo(u23), bf_hi(g23) * bf_hi(u23));
      }
      const int m16 = mt * 8 + wid * 2 + tt;
      char* hb = ws + WS_H + (size_t)m16 * 56 * 1024 + lane * 16;
      const int ft0 = 7 * jt;
      if ((jt & 1) == 0) {
        const int s0 = ft0 >> 1;
#pragma unroll
        for (int i = 0; i < 3; ++i)
          *(u32x4*)(hb + (s0 + i) * 1024) = (u32x4){hv[2 * i][0], hv[2 * i][1], hv[2 * i + 1][0], hv[2 * i + 1][1]};
        *(u32x2*)(hb + (s0 + 3) * 1024) = hv[6];
      } else {
        const int s0 = ft0 >> 1;
        *(u32x2*)(hb + s0 * 1024 + 8) = hv[0];
#pragma unroll
        for (int i = 0; i < 3; ++i)
          *(u32x4*)(hb + (s0 + 1 + i) * 1024) = (u32x4){hv[2 * i + 1][0], hv[2 * i + 1][1], hv[2 * i + 2][0], hv[2 * i + 2][1]};
      }
    }
  } else {
    const int hw = w4;
    const int wdi = bid;
    if (wdi < 224) {
      const int s2 = wdi >> 2, cq = wdi & 3;
#pragma unroll
      for (int i = 0; i < 2; ++i) {
        int n = hw + 4 * i;
        if (n < 7) {
          int sg = 64 * n + lane;
          int rho = sg / 14, ch = sg - rho * 14;
          glds16(wd + (size_t)(32 * s2) * (DM * 2) + cq * 224, rho * (DM * 2) + ch * 16, lbase + K1_LWD + n * 1024);
        }
      }
    }
    const int tok0 = mt * 128 + hw * 32;
    u32x4 xr[2][14];
#pragma unroll
    for (int tt = 0; tt < 2; ++tt)
#pragma unroll
      for (int s = 0; s < 14; ++s)
        xr[tt][s] = *(const u32x4*)(x + (size_t)(tok0 + 16 * tt + r) * (DM * 2) + s * 64 + q * 16);
    float mb[2], rs[2];
    const bf16x2 ones = {(__bf16)1.0f, (__bf16)1.0f};
#pragma unroll
    for (int tt = 0; tt < 2; ++tt) {
      float s1a = 0, s1b = 0, s2a = 0, s2b = 0;
#pragma unroll
      for (int s = 0; s < 14; ++s) {
#pragma unroll
        for (int w = 0; w < 4; w += 2) {
          const unsigned t0 = xr[tt][s][w], t1 = xr[tt][s][w + 1];
          bf16x2 v0 = __builtin_bit_cast(bf16x2, t0), v1 = __builtin_bit_cast(bf16x2, t1);
          s1a = __builtin_amdgcn_fdot2_f32_bf16(v0, ones, s1a, false);
          s2a = __builtin_amdgcn_fdot2_f32_bf16(v0, v0, s2a, false);
          s1b = __builtin_amdgcn_fdot2_f32_bf16(v1, ones, s1b, false);
          s2b = __builtin_amdgcn_fdot2_f32_bf16(v1, v1, s2b, false);
        }
      }
      float s1 = s1a + s1b, s2 = s2a + s2b;
      s1 += __shfl_xor(s1, 16); s2 += __shfl_xor(s2, 16);
      s1 += __shfl_xor(s1, 32); s2 += __shfl_xor(s2, 32);
      float mean = s1 * (1.0f / DM);
      float var = fmaxf(s2 * (1.0f / DM) - mean * mean, 0.0f);
      mb[tt] = rbf(mean);
      float den = rbf(sqrtf(rbf(rbf(var) + 1e-5f)));
      rs[tt] = 1.0f / den;
    }
    char* Aw = lds + K1_LA + (2 * hw) * 1024 + lane * 16;
    char* XT = lds + K1_LXT + hw * 2048;
#pragma unroll
    for (int c = 0; c < 7; ++c) {
#pragma unroll
      for (int ks = 0; ks < 2; ++ks) {
        const int s = 2 * c + ks;
#pragma unroll
        for (int tt = 0; tt < 2; ++tt) {
          u32x4 o;
#pragma unroll
          for (int w = 0; w < 4; ++w) {
            unsigned v = xr[tt][s][w];
            unsigned d = pk_bf16(bf_lo(v) - mb[tt], bf_hi(v) - mb[tt]);
            o[w] = pk_bf16(bf_lo(d) * rs[tt], bf_hi(d) * rs[tt]);
          }
          *(u32x4*)(Aw + (c & 1) * K1_ASLOT + ks * 8192 + tt * 1024) = o;
          if (s == jt) *(u32x4*)(XT + tt * 1024 + lane * 16) = o;
        }
      }
      if (c == 4) WAIT_V(14); else if (c == 5) WAIT_V(7); else if (c == 6) WAIT_V(0);
      BAR();
      if (c >= 1 && c <= 3) dma_chunk(c + 3);
    }
    const int qq = (lane & 15) >> 2, p = lane & 3;
    if (jt < 14) {
#pragma unroll
      for (int tt = 0; tt < 2; ++tt)
#pragma unroll
        for (int cb = 0; cb < 2; ++cb) {
          u32x2 v = tr_read(XT + tt * 1024 + ((2 * cb + (p >> 1)) * 16 + 4 * q + qq) * 16 + 8 * (p & 1));
          u32x2 o = {pk_f16(bf_lo(v[0]), bf_hi(v[0])), pk_f16(bf_lo(v[1]), bf_hi(v[1]))};
          *(u32x2*)(ws + WS_XT + ((size_t)(32 * jt + 16 * cb + r) * SEQ + tok0 + 16 * tt + 4 * q) * 2) = o;
        }
    }
    if (wdi < 224) {
      const int s2 = wdi >> 2, cq = wdi & 3;
#pragma unroll
      for (int i = 0; i < 2; ++i) {
        int ct = hw + 4 * i;
        if (ct < 7) {
          const char* bp = lds + K1_LWD + (4 * q + qq) * 224 + ct * 32 + p * 8;
          u32x2 lo = tr_read(bp), hi = tr_read(bp + 16 * 224);
          *(u32x4*)(ws + WS_WD + (size_t)(s2 * 28 + 7 * cq + ct) * 1024 + lane * 16) = (u32x4){lo[0], lo[1], hi[0], hi[1]};
        }
      }
    }
  }
}

#define K2_LDS 49152
#define CV_XS 4160
#define CV_T 16640
#define CV_TW 4608
#define CV_KR (CV_T + 4 * CV_TW)
#define CV_MISC (CV_KR + 4608)

struct cpx { float r, i; };
__device__ __forceinline__ cpx cmul(cpx a, cpx b) {
  cpx o;
  o.r = fmaf(a.r, b.r, -(a.i * b.i));
  o.i = fmaf(a.r, b.i, a.i * b.r);
  return o;
}
__device__ __forceinline__ cpx cexp_tau(float re_l2, float rev_hi, float rev_lo, float tau) {
  float mag = __builtin_amdgcn_exp2f(re_l2 * tau);
  float fr = __builtin_amdgcn_fractf(rev_hi * tau) + rev_lo * tau;
  cpx o;
  o.r = mag * __builtin_amdgcn_cosf(fr);
  o.i = mag * __builtin_amdgcn_sinf(fr);
  return o;
}

__global__ __launch_bounds__(256, 3) void k2_kernel(const char* __restrict__ Are, const char* __restrict__ Aim,
                                                    const char* __restrict__ Bre, const char* __restrict__ Bim,
                                                    const char* __restrict__ Cre, const char* __restrict__ Cim,
                                                    const char* __restrict__ logdt, const char* __restrict__ Dskip,
                                                    char* __restrict__ ws) {
  extern __shared__ __attribute__((aligned(16))) char lds[];
  const int tid = threadIdx.x, lane = tid & 63;
  const int wid = __builtin_amdgcn_readfirstlane(tid >> 6);
  const int r = lane & 15, q = lane >> 4;
  const int bid = blockIdx.x;
  if (bid < 224) {
    const int xcd = bid & 7, idx = bid >> 3;
    const int mt = 4 * xcd + (idx & 3), nt = idx >> 2;
    const u32x4* hA = (const u32x4*)(ws + WS_H) + ((size_t)(4 * mt) * 56 + 14 * wid) * 64 + lane;
    const u32x4* wB = (const u32x4*)(ws + WS_WD) + ((size_t)(14 * wid) * 28 + 4 * nt) * 64 + lane;
    f32x4 acc[4][4];
#pragma unroll
    for (int i = 0; i < 4; ++i)
#pragma unroll
      for (int j = 0; j < 4; ++j) acc[i][j] = (f32x4){0, 0, 0, 0};
    u32x4 a[2][4], b[2][4];
    auto load = [&](int buf, int s) {
#pragma unroll
      for (int i = 0; i < 4; ++i) {
        a[buf][i] = hA[(i * 56 + s) * 64];
        b[buf][i] = wB[(s * 28 + i) * 64];
      }
    };
    auto comp = [&](int buf) {
#pragma unroll
      for (int j = 0; j < 4; ++j)
#pragma unroll
        for (int i = 0; i < 4; ++i)
          acc[j][i] = __builtin_amdgcn_mfma_f32_16x16x32_bf16(__builtin_bit_cast(bf16x8, b[buf][j]),
                                                              __builtin_bit_cast(bf16x8, a[buf][i]), acc[j][i], 0, 0, 0);
    };
    load(0, 0); load(1, 1);
#pragma unroll 1
    for (int it = 0; it < 12; it += 2) {
      comp(0); load(0, it + 2);
      comp(1); load(1, it + 3);
    }
    comp(0); comp(1);
#pragma unroll
    for (int j = 0; j < 4; ++j) {
      if (j != wid) {
        int slot = wid < j ? wid : wid - 1;
#pragma unroll
        for (int i = 0; i < 4; ++i)
          *(f32x4*)(lds + ((j * 3 + slot) * 4 + i) * 1024 + lane * 16) = acc[j][i];
      }
    }
    __syncthreads();
    f32x4 o[4];
#pragma unroll
    for (int j = 0; j < 4; ++j)
      if (j == wid) {
#pragma unroll
        for (int i = 0; i < 4; ++i) o[i] = acc[j][i];
      }
#pragma unroll
    for (int sl = 0; sl < 3; ++sl)
#pragma unroll
      for (int i = 0; i < 4; ++i) o[i] += *(const f32x4*)(lds + ((wid * 3 + sl) * 4 + i) * 1024 + lane * 16);
#pragma unroll
    for (int i = 0; i < 4; ++i) {
      u32x2 v = {pk_bf16(o[i][0], o[i][1]), pk_bf16(o[i][2], o[i][3])};
      *(u32x2*)(ws + WS_YB + ((size_t)(64 * mt + 16 * i + r) * DM + 64 * nt + 16 * wid + 4 * q) * 2) = v;
    }
    return;
  }
  const int d = bid - 224;
  const char* xt = ws + WS_XT + (size_t)d * SEQ * 2;
  u32x4 xa = *(const u32x4*)(xt + tid * 16);
  u32x2 xb = {0, 0};
  if (tid < 255) xb = *(const u32x2*)(xt + tid * 16 + 16);
  const int n = d * NS + tid;
  const float dt = rbf(__expf(bf1(logdt, d)));
  const float re = dt * bf1(Are, n), im = dt * bf1(Aim, n);
  const float br = dt * bf1(Bre, n), bi = dt * bf1(Bim, n);
  const float cr = bf1(Cre, n), ci = bf1(Cim, n);
  const float dsk = bf1(Dskip, d);
  {
    char* xc = lds + tid * 16 + 32;
    *(u32x4*)(xc) = xa;
    *(u32x4*)(xc + CV_XS) = (u32x4){__builtin_amdgcn_alignbyte(xa[1], xa[0], 2), __builtin_amdgcn_alignbyte(xa[2], xa[1], 2),
                                    __builtin_amdgcn_alignbyte(xa[3], xa[2], 2), __builtin_amdgcn_alignbyte(xb[0], xa[3], 2)};
    *(u32x4*)(xc + 2 * CV_XS) = (u32x4){xa[1], xa[2], xa[3], xb[0]};
    *(u32x4*)(xc + 3 * CV_XS) = (u32x4){__builtin_amdgcn_alignbyte(xa[2], xa[1], 2), __builtin_amdgcn_alignbyte(xa[3], xa[2], 2),
                                        __builtin_amdgcn_alignbyte(xb[0], xa[3], 2), __builtin_amdgcn_alignbyte(xb[1], xb[0], 2)};
    if (tid == 0) {
      u32x4 z = {0, 0, 0, 0};
      *(u32x4*)(lds) = z; *(u32x4*)(lds + 16) = z;
      *(u32x4*)(lds + CV_XS) = z; *(u32x4*)(lds + CV_XS + 16) = (u32x4){0, 0, 0, xa[0] << 16};
      *(u32x4*)(lds + 2 * CV_XS) = z; *(u32x4*)(lds + 2 * CV_XS + 16) = (u32x4){0, 0, 0, xa[0]};
      *(u32x4*)(lds + 3 * CV_XS) = z;
      *(u32x4*)(lds + 3 * CV_XS + 16) = (u32x4){0, 0, xa[0] << 16, __builtin_amdgcn_alignbyte(xa[1], xa[0], 2)};
    }
    if (tid < 32) *(u32x4*)(lds + CV_KR + 4096 + tid * 16) = (u32x4){0, 0, 0, 0};
  }
  {
    cpx CB; CB.r = cr * br - ci * bi; CB.i = cr * bi + ci * br;
    const float re_l2 = re * 1.44269504088896340736f;
    const float rev_hi = im * 0.15915494309189535f;
    const float rev_lo = fmaf(im, 0.15915494309189535f, -rev_hi) + im * 6.4206e-9f * 0.0f;
    const float sig = fmaxf(1.0f, __builtin_amdgcn_exp2f(re_l2 * 120.0f));
    const float gmx = fmaxf(1.0f, __builtin_amdgcn_exp2f(re_l2 * 2048.0f));
    const float cb1 = (fabsf(CB.r) + fabsf(CB.i)) * gmx;
    float mx = cb1 * sig, sm = cb1;
#pragma unroll
    for (int o = 32; o; o >>= 1) { mx = fmaxf(mx, __shfl_xor(mx, o)); sm += __shfl_xor(sm, o); }
    const int ew = __builtin_amdgcn_frexp_expf(mx);
    const float scw = __builtin_amdgcn_ldexpf(1.0f, 15 - ew);
    const float unsc = __builtin_amdgcn_ldexpf(1.0f, ew - 30);
    if (lane == 0) *(float*)(lds + CV_MISC + wid * 4) = sm;
    const cpx z1 = cexp_tau(re_l2, rev_hi, rev_lo, 1.0f);
    const cpx z8 = cexp_tau(re_l2, rev_hi, rev_lo, 8.0f);
    const cpx z128 = cexp_tau(re_l2, rev_hi, rev_lo, 128.0f);
    char* T = lds + CV_T + wid * CV_TW;
    char* Tw = T + lane * 4;
    const char* Tr = T + r * 288 + q * 16;
    {
      cpx e; e.r = 32768.0f / sig; e.i = 0.0f;
#pragma unroll
      for (int j = 0; j < 16; ++j) {
        *(unsigned*)(Tw + j * 288) = pk_f16(e.r, e.i);
        if (j < 15) e = cmul(e, z8);
      }
    }
    __builtin_amdgcn_wave_barrier();
    f16x8 E[4];
#pragma unroll
    for (int s = 0; s < 4; ++s) E[s] = *(const f16x8*)(Tr + s * 64);
    __builtin_amdgcn_wave_barrier();
    cpx G0; G0.r = CB.r * (sig * scw); G0.i = CB.i * (sig * scw);
    const cpx z2 = cmul(z1, z1), z4 = cmul(z2, z2);
    const cpx z256 = cmul(z128, z128), z512 = cmul(z256, z256), z1024 = cmul(z512, z512);
    f32x4 kacc[3];
    auto put = [&](int row, cpx g) { *(unsigned*)(Tw + row * 288) = pk_f16(g.r, -g.i); };
    auto mm = [&](int t) {
      __builtin_amdgcn_wave_barrier();
      f32x4 a = {0, 0, 0, 0};
#pragma unroll
      for (int s = 0; s < 4; ++s) {
        f16x8 g = *(const f16x8*)(Tr + s * 64);
        a = __builtin_amdgcn_mfma_f32_16x16x32_f16(E[s], g, a, 0, 0, 0);
      }
      kacc[t] = a;
      __builtin_amdgcn_wave_barrier();
    };
    {
      cpx g = G0;
#pragma unroll
      for (int i = 0; i < 8; ++i) {
        put(i, g); put(8 + i, cmul(g, z128));
        if (i < 7) g = cmul(g, z1);
      }
    }
    mm(0);
    {
      cpx g = cmul(G0, z256);
#pragma unroll
      for (int i = 0; i < 4; ++i) {
        put(i, g); put(4 + i, cmul(g, z128));
        if (i < 3) g = cmul(g, z2);
      }
      cpx ga = cmul(G0, z512), gb = cmul(ga, z4);
#pragma unroll
      for (int i = 0; i < 4; ++i) {
        put(8 + 2 * i, ga); put(9 + 2 * i, gb);
        if (i < 3) { ga = cmul(ga, z128); gb = cmul(gb, z128); }
      }
    }
    mm(1);
    {
      cpx g = cmul(G0, z1024);
#pragma unroll
      for (int i = 0; i < 9; ++i) {
        put(i, g);
        if (i < 8) g = cmul(g, z128);
      }
    }
    mm(2);
#pragma unroll
    for (int t = 0; t < 3; ++t) *(f32x4*)(T + ((16 * t + r) * 16 + 4 * q) * 4) = kacc[t] * unsc;
  }
  __syncthreads();
  float kun;
  {
    const float* ms = (const float*)(lds + CV_MISC);
    const float tot = (ms[0] + ms[1]) + (ms[2] + ms[3]);
    const int ek = __builtin_amdgcn_frexp_expf(tot);
    const float ksc = __builtin_amdgcn_ldexpf(1.0f, 15 - ek);
    kun = __builtin_amdgcn_ldexpf(1.0f, ek - 15);
    float kv[8];
#pragma unroll
    for (int jj = 0; jj < 8; ++jj) {
      const int lag = 2047 - 8 * tid - jj;
      const int lb = (int)rbf((float)lag);
      int seg = (31 - __builtin_clz(lb | 128)) - 7;
      int o = seg ? lb - (128 << seg) : lb;
      int row = (seg ? 8 + 8 * seg : 0) + ((o & 7) >> seg) + (8 >> seg) * (o >> 7);
      int ix = (row * 16 + ((lb >> 3) & 15)) * 4;
      const char* kp = lds + CV_T + ix;
      kv[jj] = ((*(const float*)kp + *(const float*)(kp + CV_TW)) + (*(const float*)(kp + 2 * CV_TW) + *(const float*)(kp + 3 * CV_TW))) * ksc;
    }
    *(u32x4*)(lds + CV_KR + tid * 16) = (u32x4){pk_f16(kv[0], kv[1]), pk_f16(kv[2], kv[3]), pk_f16(kv[4], kv[5]), pk_f16(kv[6], kv[7])};
  }
  __syncthreads();
  f32x4 acc[8];
#pragma unroll
  for (int i = 0; i < 8; ++i) acc[i] = (f32x4){0, 0, 0, 0};
  {
    const int e0 = 8 * q + r + 1;
    const char* xp = lds + (e0 & 3) * CV_XS + (e0 & ~3) * 2;
#pragma unroll 1
    for (int rr = 0; rr < 2; ++rr) {
      const int m0 = 2 * wid + rr;
      const char* kp = lds + CV_KR + (2032 + 32 * m0 - 16 * r + 8 * q) * 2;
      f16x8 G[8];
#pragma unroll
      for (int dl = 0; dl < 8; ++dl) G[dl] = *(const f16x8*)(kp - dl * 512);
#pragma unroll
      for (int i = 0; i < 8; ++i) {
        const char* fp = xp + (m0 + 8 * i) * 64;
        u32x2 f0 = *(const u32x2*)fp, f1 = *(const u32x2*)(fp + 8);
        f16x8 Fr = __builtin_bit_cast(f16x8, (u32x4){f0[0], f0[1], f1[0], f1[1]});
#pragma unroll
        for (int nn = i; nn < 8; ++nn) acc[nn] = __builtin_amdgcn_mfma_f32_16x16x32_f16(Fr, G[nn - i], acc[nn], 0, 0, 0);
      }
    }
  }
  u32x2 xo[2];
#pragma unroll
  for (int i = 0; i < 2; ++i) xo[i] = *(const u32x2*)(lds + (256 * (2 * wid + i) + 16 * r + 4 * q + 16) * 2);
  __syncthreads();
#pragma unroll
  for (int j = 0; j < 4; ++j) {
    if (j != wid) {
      int slot = wid < j ? wid : wid - 1;
      *(f32x4*)(lds + ((j * 3 + slot) * 2 + 0) * 1024 + lane * 16) = acc[2 * j];
      *(f32x4*)(lds + ((j * 3 + slot) * 2 + 1) * 1024 + lane * 16) = acc[2 * j + 1];
    }
  }
  __syncthreads();
  f32x4 o[2];
#pragma unroll
  for (int j = 0; j < 4; ++j)
    if (j == wid) { o[0] = acc[2 * j]; o[1] = acc[2 * j + 1]; }
#pragma unroll
  for (int sl = 0; sl < 3; ++sl)
#pragma unroll
    for (int i = 0; i < 2; ++i) o[i] += *(const f32x4*)(lds + ((wid * 3 + sl) * 2 + i) * 1024 + lane * 16);
#pragma unroll
  for (int i = 0; i < 2; ++i) {
    f16x4 xv = __builtin_bit_cast(f16x4, xo[i]);
    float y[4];
#pragma unroll
    for (int k = 0; k < 4; ++k) y[k] = rbf(o[i][k] * kun) + rbf((float)xv[k] * dsk);
    u32x2 v = {pk_bf16(y[0], y[1]), pk_bf16(y[2], y[3])};
    *(u32x2*)(ws + WS_YT + ((size_t)d * SEQ + 256 * (2 * wid + i) + 16 * r + 4 * q) * 2) = v;
  }
}

__global__ __launch_bounds__(256) void k3_kernel(const char* __restrict__ x, const char* __restrict__ ws, char* __restrict__ out) {
  __shared__ __attribute__((aligned(16))) char lds[64 * 144];
  const int tid = threadIdx.x, lane = tid & 63, wid = tid >> 6;
  const int r = lane & 15, q = lane >> 4, qq = (lane & 15) >> 2, p = lane & 3;
  const int bid = blockIdx.x;
  const int xcd = bid & 7, idx = bid >> 3;
  const int mt = 4 * xcd + (idx & 3), nt = idx >> 2;
  u32x2 xv[4], bv[4];
#pragma unroll
  for (int cb = 0; cb < 4; ++cb) {
    size_t o = ((size_t)(64 * mt + 16 * wid + r) * DM + 64 * nt + 16 * cb + 4 * q) * 2;
    xv[cb] = *(const u32x2*)(x + o);
    bv[cb] = *(const u32x2*)(ws + WS_YB + o);
  }
#pragma unroll
  for (int i = 0; i < 2; ++i) {
    int c = tid + 256 * i;
    u32x4 v = *(const u32x4*)(ws + WS_YT + ((size_t)(64 * nt + (c >> 3)) * SEQ + 64 * mt) * 2 + (c & 7) * 16);
    *(u32x4*)(lds + (c >> 3) * 144 + (c & 7) * 16) = v;
  }
  __syncthreads();
#pragma unroll
  for (int cb = 0; cb < 4; ++cb) {
    u32x2 yv = tr_read(lds + (16 * cb + 4 * q + qq) * 144 + (16 * wid + 4 * p) * 2);
    u32x2 o;
#pragma unroll
    for (int k = 0; k < 2; ++k) {
      float lo = rbf(bf_lo(xv[cb][k]) + bf_lo(yv[k])) + bf_lo(bv[cb][k]);
      float hi = rbf(bf_hi(xv[cb][k]) + bf_hi(yv[k])) + bf_hi(bv[cb][k]);
      o[k] = pk_bf16(lo, hi);
    }
    *(u32x2*)(out + ((size_t)(64 * mt + 16 * wid + r) * DM + 64 * nt + 16 * cb + 4 * q) * 2) = o;
  }
}

extern "C" void kernel_launch(void* const* d_in, const int* in_sizes, int n_in, void* d_out, int out_size, void* d_ws,
                              size_t ws_size, hipStream_t stream) {
  const char* x = (const char*)d_in[0];
  char* ws = (char*)d_ws;
  (void)hipFuncSetAttribute((const void*)k1_kernel, hipFuncAttributeMaxDynamicSharedMemorySize, K1_LDS);
  (void)hipFuncSetAttribute((const void*)k2_kernel, hipFuncAttributeMaxDynamicSharedMemorySize, K2_LDS);
  k1_kernel<<<256, 512, K1_LDS, stream>>>(x, (const char*)d_in[10], (const char*)d_in[11], (const char*)d_in[12], ws);
  k2_kernel<<<224 + 448, 256, K2_LDS, stream>>>((const char*)d_in[1], (const char*)d_in[2], (const char*)d_in[3],
                                                (const char*)d_in[4], (const char*)d_in[5], (const char*)d_in[6],
                                                (const char*)d_in[7], (const char*)d_in[8], ws);
  k3_kernel<<<224, 256, 0, stream>>>(x, ws, (char*)d_out);
}
```

```cpp
#include <hip/hip_runtime.h>
#include <hip/hip_bf16.h>
#include <stdint.h>

typedef __attribute__((ext_vector_type(8))) __bf16 bf16x8;
typedef __attribute__((ext_vector_type(2))) __bf16 bf16x2;
typedef __attribute__((ext_vector_type(8))) _Float16 f16x8;
typedef __attribute__((ext_vector_type(4))) _Float16 f16x4;
typedef __attribute__((ext_vector_type(2))) _Float16 f16x2;
typedef __attribute__((ext_vector_type(4))) float f32x4;
typedef __attribute__((ext_vector_type(2))) float f32x2;
typedef __attribute__((ext_vector_type(4))) unsigned u32x4;
typedef __attribute__((ext_vector_type(2))) unsigned u32x2;
typedef __attribute__((ext_vector_type(4))) short s16x4;
#define LAS __attribute__((address_space(3)))
#define GAS __attribute__((address_space(1)))
#define RLX_AGENT __ATOMIC_RELAXED, __HIP_MEMORY_SCOPE_AGENT

#define SEQ 2048
#define DM 448
#define NS 256
#define FF 1792

#define WS_H 0u
#define WS_WD 7340032u
#define WS_XT (WS_WD + 1605632u)
#define WS_YT (WS_XT + 1835008u)
#define WS_XN (WS_YT + 1835008u)
#define WS_KR (WS_XN + 1835008u)
#define WS_KS (WS_KR + 1835008u)
#define WS_CNT (WS_KS + 2048u)

__device__ __forceinline__ unsigned pk_bf16(float a, float b) {
  bf16x2 v = {(__bf16)a, (__bf16)b};
  return __builtin_bit_cast(unsigned, v);
}
__device__ __forceinline__ unsigned pk_bf16o(float a, float b) {
  unsigned r;
  asm("v_cvt_pk_bf16_f32 %0, %1, %2" : "=v"(r) : "v"(a), "v"(b));
  return r;
}
__device__ __forceinline__ unsigned pk_f16(float a, float b) {
  f16x2 v = {(_Float16)a, (_Float16)b};
  return __builtin_bit_cast(unsigned, v);
}
__device__ __forceinline__ float bf_lo(unsigned u) { return __uint_as_float(u << 16); }
__device__ __forceinline__ float bf_hi(unsigned u) { return __uint_as_float(u & 0xffff0000u); }
__device__ __forceinline__ float rbf(float x) { return (float)(__bf16)x; }
__device__ __forceinline__ float bf1(const void* p, int i) {
  return __uint_as_float(((unsigned)((const unsigned short*)p)[i]) << 16);
}
__device__ __forceinline__ u32x2 tr_read(const char* lds) {
  s16x4 v = __builtin_amdgcn_ds_read_tr16_b64_v4i16((LAS s16x4*)lds);
  return __builtin_bit_cast(u32x2, v);
}
#define ST(kid, i)
#define WAIT_V(n) asm volatile("s_waitcnt vmcnt(" #n ")" ::: "memory")
#define BAR() asm volatile("s_waitcnt lgkmcnt(0)\n\ts_barrier" ::: "memory")
__device__ __forceinline__ void glds16(const char* sbase, unsigned voff, unsigned lds_dst) {
  unsigned keep;
  asm volatile("s_mov_b32 %0, m0\n\ts_mov_b32 m0, %3\n\ts_nop 0\n\tglobal_load_lds_dwordx4 %1, %2\n\ts_mov_b32 m0, %0"
               : "=&s"(keep) : "v"(voff), "s"(sbase), "s"(lds_dst) : "memory");
}
__device__ __forceinline__ float sum_q(float v) {
  unsigned u = __float_as_uint(v);
  auto a = __builtin_amdgcn_permlane16_swap(u, u, false, false);
  float s = __uint_as_float(a[0]) + __uint_as_float(a[1]);
  u = __float_as_uint(s);
  auto b = __builtin_amdgcn_permlane32_swap(u, u, false, false);
  return __uint_as_float(b[0]) + __uint_as_float(b[1]);
}

#define KG_TW 4608
#define KG_KC (4 * KG_TW)
#define KG_MISC (KG_KC + 1536)
#define K0_LDS 28672

__device__ __forceinline__ f32x2 cmul2(f32x2 a, f32x2 b, f32x2 bs) {
  f32x2 t = a.xx * b;
  return __builtin_elementwise_fma(a.yy, bs, t);
}
__device__ __forceinline__ f32x2 rot(f32x2 b) { return (f32x2){-b.y, b.x}; }
__device__ __forceinline__ f32x2 csq(f32x2 a) {
  float r = fmaf(a.x, a.x, -(a.y * a.y)), i = (a.x + a.x) * a.y;
  asm volatile("" : "+v"(r), "+v"(i));
  return (f32x2){r, i};
}
__device__ __forceinline__ f32x2 cexp_tau(float re_l2, float rev_hi, float rev_lo, float tau) {
  float mag = __builtin_amdgcn_exp2f(re_l2 * tau);
  float fr = __builtin_amdgcn_fractf(rev_hi * tau) + rev_lo * tau;
  return (f32x2){mag * __builtin_amdgcn_cosf(fr), mag * __builtin_amdgcn_sinf(fr)};
}
#define DPP(v, ctrl) __int_as_float(__builtin_amdgcn_update_dpp(0, __float_as_int(v), ctrl, 0xf, 0xf, false))

__global__ __launch_bounds__(256) void k0_kernel(const char* __restrict__ x, const char* __restrict__ Are, const char* __restrict__ Aim,
                                                 const char* __restrict__ Bre, const char* __restrict__ Bim,
                                                 const char* __restrict__ Cre, const char* __restrict__ Cim,
                                                 const char* __restrict__ logdt, const char* __restrict__ wd, char* __restrict__ ws) {
  extern __shared__ __attribute__((aligned(16))) char lds[];
  const int tid = threadIdx.x, lane = tid & 63;
  const int wid = __builtin_amdgcn_readfirstlane(tid >> 6);
  const int r = lane & 15, q = lane >> 4, qq = (lane & 15) >> 2, p = lane & 3;
  const int bid = blockIdx.x;
  if (bid >= 576) {
    if (bid == 576 && tid < 7) *(unsigned*)(ws + WS_CNT + tid * 256) = 0u;
    const unsigned lbase = (unsigned)(size_t)(LAS char*)lds;
    const int u = (bid - 576) * 4 + wid, s2 = u >> 2, cq = u & 3;
    const char* src = wd + (size_t)(32 * s2) * (DM * 2) + cq * 224;
#pragma unroll
    for (int i = 0; i < 7; ++i) glds16(src + i * 32, (lane >> 1) * (DM * 2) + (lane & 1) * 16, lbase + wid * 7168 + i * 1024);
    WAIT_V(0);
    const char* bp = lds + wid * 7168 + (4 * q + qq) * 32 + p * 8;
#pragma unroll
    for (int i = 0; i < 7; ++i) {
      u32x2 lo = tr_read(bp + i * 1024), hi = tr_read(bp + i * 1024 + 512);
      *(u32x4*)(ws + WS_WD + (size_t)(s2 * 28 + 7 * cq + i) * 1024 + lane * 16) = (u32x4){lo[0], lo[1], hi[0], hi[1]};
    }
    return;
  }
  if (bid >= 448) {
    const int tok0 = (bid - 448) * 16;
    const size_t rowo = (size_t)(tok0 + r) * (DM * 2) + q * 16 + wid * 64;
    u32x4 xr[4];
#pragma unroll
    for (int i = 0; i < 3; ++i) xr[i] = *(const u32x4*)(x + rowo + i * 256);
    xr[3] = (u32x4){0, 0, 0, 0};
    if (wid < 2) xr[3] = *(const u32x4*)(x + rowo + 768);
    float s1 = 0, s2 = 0;
#pragma unroll
    for (int i = 0; i < 4; ++i)
#pragma unroll
      for (int w = 0; w < 4; ++w) {
        const unsigned v = xr[i][w];
        const float lo = bf_lo(v), hi = bf_hi(v);
        s1 += lo + hi;
        s2 = fmaf(lo, lo, fmaf(hi, hi, s2));
      }
    s1 = sum_q(s1); s2 = sum_q(s2);
    if (q == 0) *(f32x2*)(lds + (wid * 16 + r) * 8) = (f32x2){s1, s2};
    __syncthreads();
    float t1 = 0, t2 = 0;
#pragma unroll
    for (int w = 0; w < 4; ++w) { const f32x2 v = *(const f32x2*)(lds + (w * 16 + r) * 8); t1 += v.x; t2 += v.y; }
    const float mean = t1 * (1.0f / DM);
    const float var = fmaxf(t2 * (1.0f / DM) - mean * mean, 0.0f);
    const float mb = rbf(mean);
    const float rs = 1.0f / rbf(sqrtf(rbf(rbf(var) + 1e-5f)));
    char* XT = lds + 1024 + wid * 4096;
#pragma unroll
    for (int i = 0; i < 4; ++i) {
      if (i < 3 || wid < 2) {
        u32x4 o;
#pragma unroll
        for (int w = 0; w < 4; ++w) {
          const unsigned v = xr[i][w];
          const unsigned d = pk_bf16o(bf_lo(v) - mb, bf_hi(v) - mb);
          o[w] = pk_bf16o(bf_lo(d) * rs, bf_hi(d) * rs);
        }
        *(u32x4*)(ws + WS_XN + rowo + i * 256) = o;
        *(u32x4*)(XT + i * 1024 + lane * 16) = o;
      }
    }
    __builtin_amdgcn_wave_barrier();
#pragma unroll
    for (int i = 0; i < 4; ++i) {
      if (i < 3 || wid < 2) {
#pragma unroll
        for (int cb = 0; cb < 2; ++cb) {
          u32x2 v = tr_read(XT + i * 1024 + ((2 * cb + (p >> 1)) * 16 + 4 * q + qq) * 16 + 8 * (p & 1));
          u32x2 o = {pk_f16(bf_lo(v[0]), bf_hi(v[0])), pk_f16(bf_lo(v[1]), bf_hi(v[1]))};
          *(u32x2*)(ws + WS_XT + ((size_t)(32 * (wid + 4 * i) + 16 * cb + r) * SEQ + tok0 + 4 * q) * 2) = o;
        }
      }
    }
    return;
  }
  const int d = bid;
  ST(2, 0);
  const int n = d * NS + tid;
  const float a_re = bf1(Are, n), a_im = bf1(Aim, n), b_re = bf1(Bre, n), b_im = bf1(Bim, n), cr = bf1(Cre, n), ci = bf1(Cim, n);
  const float ldt = bf1(logdt, d);
  u32x4 kidx;
#pragma unroll
  for (int w = 0; w < 4; ++w) {
    unsigned e[2];
#pragma unroll
    for (int h = 0; h < 2; ++h) {
      const int lag = 2047 - 8 * tid - 2 * w - h;
      const int lb = (int)rbf((float)lag);
      const int seg = (31 - __builtin_clz(lb | 128)) - 7;
      const int o = lb & ~(seg ? (128 << seg) : 0);
      const int row = (seg ? 8 + 8 * seg : 0) + ((o & 7) >> seg) + ((o >> 7) << 3 >> seg);
      e[h] = (row * 16 + ((lb >> 3) & 15)) * 2;
    }
    kidx[w] = e[0] | (e[1] << 16);
  }
  asm volatile("" : "+v"(kidx));
  {
    const float dt = rbf(__expf(ldt));
    const float re = dt * a_re, im = dt * a_im, br = dt * b_re, bi = dt * b_im;
    const float CBr = cr * br - ci * bi, CBi = cr * bi + ci * br;
    const float re_l2 = re * 1.44269504088896340736f;
    const float rev_hi = im * 0.15915494309189535f;
    const float rev_lo = fmaf(im, 0.15915494309189535f, -rev_hi) + im * 6.4206e-9f;
    const float sig = fmaxf(1.0f, __builtin_amdgcn_exp2f(re_l2 * 120.0f));
    const float gmx = fmaxf(1.0f, __builtin_amdgcn_exp2f(re_l2 * 2048.0f));
    const float cb1 = (fabsf(CBr) + fabsf(CBi)) * gmx;
    float mx = cb1 * sig, sm = cb1;
    mx = fmaxf(mx, DPP(mx, 0x111)); sm += DPP(sm, 0x111);
    mx = fmaxf(mx, DPP(mx, 0x112)); sm += DPP(sm, 0x112);
    mx = fmaxf(mx, DPP(mx, 0x114)); sm += DPP(sm, 0x114);
    mx = fmaxf(mx, DPP(mx, 0x118)); sm += DPP(sm, 0x118);
    mx = fmaxf(mx, DPP(mx, 0x142)); sm += DPP(sm, 0x142);
    mx = fmaxf(mx, DPP(mx, 0x143)); sm += DPP(sm, 0x143);
    if (lane == 63) *(float*)(lds + KG_MISC + wid * 4) = sm;
    mx = __int_as_float(__builtin_amdgcn_readlane(__float_as_int(mx), 63));
    const int ew = __builtin_amdgcn_frexp_expf(mx);
    const float scw = __builtin_amdgcn_ldexpf(sig, 15 - ew);
    const float unsc = __builtin_amdgcn_ldexpf(1.0f, ew - 30);
    const f32x2 z1 = cexp_tau(re_l2, rev_hi, rev_lo, 1.0f);
    const f32x2 z8 = cexp_tau(re_l2, rev_hi, rev_lo, 8.0f);
    const f32x2 z128 = cexp_tau(re_l2, rev_hi, rev_lo, 128.0f);
    ST(2, 1);
    char* T = lds + wid * KG_TW;
    char* Tw = T + lane * 4;
    const char* Tr = T + r * 288 + q * 16;
    auto put = [&](int row, f32x2 g) { *(unsigned*)(Tw + row * 288) = pk_f16(g.x, g.y); };
    {
      const f32x2 z8s = rot(z8);
      f32x2 e = {32768.0f * __builtin_amdgcn_rcpf(sig), 0.0f};
#pragma unroll
      for (int j = 0; j < 16; ++j) {
        put(j, e);
        if (j < 15) e = cmul2(e, z8, z8s);
      }
    }
    __builtin_amdgcn_wave_barrier();
    f16x8 E[4];
#pragma unroll
    for (int s = 0; s < 4; ++s) E[s] = *(const f16x8*)(Tr + s * 64);
    __builtin_amdgcn_wave_barrier();
    const f32x2 y1 = {z1.x, -z1.y}, y128 = {z128.x, -z128.y};
    const f32x2 y1s = rot(y1), y128s = rot(y128);
    const f32x2 H0 = {CBr * scw, -CBi * scw};
    const f32x2 y2 = csq(y1), y4 = csq(y2), y256 = csq(y128), y512 = csq(y256), y1024 = csq(y512);
    f32x4 kacc[3];
    auto mm = [&](int t) {
      __builtin_amdgcn_wave_barrier();
      f32x4 a = {0, 0, 0, 0};
#pragma unroll
      for (int s = 0; s < 4; ++s) {
        f16x8 g = *(const f16x8*)(Tr + s * 64);
        a = __builtin_amdgcn_mfma_f32_16x16x32_f16(E[s], g, a, 0, 0, 0);
      }
      kacc[t] = a;
      __builtin_amdgcn_wave_barrier();
    };
    {
      f32x2 g = H0;
#pragma unroll
      for (int i = 0; i < 8; ++i) {
        put(i, g); put(8 + i, cmul2(g, y128, y128s));
        if (i < 7) g = cmul2(g, y1, y1s);
      }
    }
    mm(0);
    {
      const f32x2 y2s = rot(y2);
      f32x2 g = cmul2(H0, y256, rot(y256));
#pragma unroll
      for (int i = 0; i < 4; ++i) {
        put(i, g); put(4 + i, cmul2(g, y128, y128s));
        if (i < 3) g = cmul2(g, y2, y2s);
      }
      f32x2 ga = cmul2(H0, y512, rot(y512)), gb = cmul2(ga, y4, rot(y4));
#pragma unroll
      for (int i = 0; i < 4; ++i) {
        put(8 + 2 * i, ga); put(9 + 2 * i, gb);
        if (i < 3) { ga = cmul2(ga, y128, y128s); gb = cmul2(gb, y128, y128s); }
      }
    }
    mm(1);
    {
      f32x2 g = cmul2(H0, y1024, rot(y1024));
#pragma unroll
      for (int i = 0; i < 9; ++i) {
        put(i, g);
        if (i < 8) g = cmul2(g, y128, y128s);
      }
    }
    mm(2);
#pragma unroll
    for (int t = 0; t < 3; ++t) *(f32x4*)(T + ((16 * t + r) * 16 + 4 * q) * 4) = kacc[t] * unsc;
  }
  ST(2, 2);
  __syncthreads();
  {
    const f32x4 ms = *(const f32x4*)(lds + KG_MISC);
    const int ek = __builtin_amdgcn_frexp_expf((ms[0] + ms[1]) + (ms[2] + ms[3]));
    if (tid == 255) *(float*)(ws + WS_KS + d * 4) = __builtin_amdgcn_ldexpf(1.0f, ek - 15);
    if (tid < 192) {
      const float ksc = __builtin_amdgcn_ldexpf(1.0f, 15 - ek);
      const char* kp = lds + tid * 16;
      f32x4 v = ((*(const f32x4*)kp + *(const f32x4*)(kp + KG_TW)) + (*(const f32x4*)(kp + 2 * KG_TW) + *(const f32x4*)(kp + 3 * KG_TW))) * ksc;
      *(u32x2*)(lds + KG_KC + tid * 8) = (u32x2){pk_f16(v[0], v[1]), pk_f16(v[2], v[3])};
    }
  }
  __syncthreads();
  {
    const char* kc = lds + KG_KC;
    u32x4 o;
#pragma unroll
    for (int w = 0; w < 4; ++w) {
      const unsigned iv = kidx[w];
      f16x2 h;
      h.x = *(const _Float16*)(kc + (iv & 0xffffu));
      h.y = *(const _Float16*)(kc + (iv >> 16));
      o[w] = __builtin_bit_cast(unsigned, h);
    }
    *(u32x4*)(ws + WS_KR + (size_t)d * 4096 + tid * 16) = o;
  }
  ST(2, 3);
}

#define K1_SLOT 49152
#define K1_LDS (3 * K1_SLOT)

__global__ __launch_bounds__(768, 3) void k1_kernel(const char* __restrict__ wg, const char* __restrict__ wu, char* __restrict__ ws) {
  extern __shared__ __attribute__((aligned(16))) char lds[];
  const int tid = threadIdx.x, lane = tid & 63;
  const int wid = __builtin_amdgcn_readfirstlane(tid >> 6);
  const int bid = blockIdx.x;
  const unsigned lbase = (unsigned)(size_t)(LAS char*)lds;
  const int r = lane & 15, q = lane >> 4, qq = (lane & 15) >> 2, p = lane & 3;
  const int xcd = bid & 7, idx = bid >> 3;
  const int mt = 4 * (xcd & 3) + (idx & 3);
  const int jt = 7 * (xcd >> 2) + (idx >> 2);
  const int m = (wid >> 2) & 1, n = wid & 3;
  ST(0, 0);
  if (wid >= 8) {
    const int pw = wid - 8;
    const unsigned aoff = (lane >> 3) * (DM * 2) + (((lane & 7) ^ (lane >> 3)) * 16);
    const char* xsrc = ws + WS_XN + (size_t)(mt * 128 + pw * 16) * (DM * 2);
    const unsigned boff = (8 * pw + q) * (FF * 2) + ((((r >> 1) ^ ((4 * (pw & 1) + q) & 7)) * 2 + (r & 1)) * 16);
    const char* gsrc = wg + jt * 256;
    const char* usrc = wu + jt * 256;
    auto dma_chunk = [&](int c, int slot) {
      const unsigned dst = lbase + slot * K1_SLOT + pw * 1024;
      const size_t wo = (size_t)c * (64 * FF * 2);
#pragma unroll
      for (int h = 0; h < 2; ++h) {
        glds16(xsrc + c * 128 + h * 64 * DM * 2, aoff, dst + pw * 1024 + h * 8192);
        glds16(xsrc + c * 128 + h * 64 * DM * 2 + 8 * DM * 2, aoff, dst + pw * 1024 + h * 8192 + 1024);
        glds16(gsrc + wo + h * 4 * FF * 2, boff, dst + 16384 + h * 4096);
        glds16(usrc + wo + h * 4 * FF * 2, boff, dst + 32768 + h * 4096);
        glds16(gsrc + wo + (32 + h * 4) * FF * 2, boff, dst + 16384 + 8192 + h * 4096);
        glds16(usrc + wo + (32 + h * 4) * FF * 2, boff, dst + 32768 + 8192 + h * 4096);
      }
    };
    dma_chunk(0, 0); dma_chunk(1, 1);
    WAIT_V(12);
    BAR();
    dma_chunk(2, 2);
    int slot = 0;
#pragma unroll 1
    for (int c = 0; c < 6; ++c) {
      if (c < 5) WAIT_V(12); else WAIT_V(0);
      BAR();
      if (c < 4) dma_chunk(c + 3, slot);
      slot = slot == 2 ? 0 : slot + 1;
    }
    return;
  }
  f32x4 acc[4][4];
#pragma unroll
  for (int i = 0; i < 4; ++i)
#pragma unroll
    for (int j = 0; j < 4; ++j) acc[i][j] = (f32x4){0, 0, 0, 0};
  const int x7 = (4 * q + qq) & 7;
  const int brd0 = 16384 + (4 * q + qq) * 256 + (((2 * n) ^ x7) * 32) + p * 8;
  const int brd1 = brd0 ^ 32;
  const int ard0 = (4 * m) * 2048 + (r >> 3) * 1024 + (r & 7) * 128 + ((q ^ (r & 7)) * 16);
  const int ard1 = ard0 ^ 64;
  u32x4 a[2][4], b[2][4];
  auto rdA = [&](int ks, const char* S, int i) { a[ks][i] = *(const u32x4*)(S + (ks ? ard1 : ard0) + i * 2048); };
  auto rdB = [&](int ks, const char* S, int t) {
    const char* bp = S + ((t & 1) ? brd1 : brd0) + (t >> 1) * 16384 + ks * 8192;
    u32x2 lo = tr_read(bp), hi = tr_read(bp + 4096);
    b[ks][t] = (u32x4){lo[0], lo[1], hi[0], hi[1]};
  };
  auto mm = [&](int ks, const char* S) {
#pragma unroll
    for (int t = 0; t < 4; ++t) {
#pragma unroll
      for (int i = 0; i < 4; ++i) {
        acc[t][i] = __builtin_amdgcn_mfma_f32_16x16x32_bf16(__builtin_bit_cast(bf16x8, b[ks][t]), __builtin_bit_cast(bf16x8, a[ks][i]), acc[t][i], 0, 0, 0);
        const int st = 4 * t + i;
        if (st < 8) { if (st & 1) rdB(1 - ks, S, st >> 1); else rdA(1 - ks, S, st >> 1); }
        __builtin_amdgcn_sched_barrier(0);
      }
    }
    __builtin_amdgcn_s_waitcnt(0xC07F);
  };
  BAR();
  ST(0, 2);
#pragma unroll
  for (int t = 0; t < 4; ++t) { rdA(0, lds, t); rdB(0, lds, t); }
  __builtin_amdgcn_s_waitcnt(0xC07F);
  int slot = 0;
#pragma unroll 1
  for (int c = 0; c < 7; ++c) {
    mm(0, lds + slot * K1_SLOT);
    if (c < 6) {
      BAR();
      ST(0, 3 + c);
      slot = slot == 2 ? 0 : slot + 1;
    }
    mm(1, lds + slot * K1_SLOT);
  }
  ST(0, 9);
#pragma unroll
  for (int i = 0; i < 4; ++i) {
    u32x4 hv;
#pragma unroll
    for (int t = 0; t < 2; ++t) {
      const f32x4 g = acc[t][i], u = acc[t + 2][i];
      unsigned g01 = pk_bf16o(g[0], g[1]), g23 = pk_bf16o(g[2], g[3]);
      unsigned u01 = pk_bf16o(u[0], u[1]), u23 = pk_bf16o(u[2], u[3]);
      hv[2 * t] = pk_bf16(bf_lo(g01) * bf_lo(u01), bf_hi(g01) * bf_hi(u01));
      hv[2 * t + 1] = pk_bf16(bf_lo(g23) * bf_lo(u23), bf_hi(g23) * bf_hi(u23));
    }
    *(u32x4*)(ws + WS_H + ((size_t)(mt * 8 + 4 * m + i) * 56 + 4 * jt + n) * 1024 + lane * 16) = hv;
  }
  ST(0, 10);
}

#define K2_LDS 49152
#define CV_XS 4160
#define CV_KR 32768

__global__ __launch_bounds__(256, 3) void k2_kernel(const char* __restrict__ Dskip, const char* __restrict__ x, char* __restrict__ out, char* ws) {
  extern __shared__ __attribute__((aligned(16))) char lds[];
  const int tid = threadIdx.x, lane = tid & 63;
  const int wid = __builtin_amdgcn_readfirstlane(tid >> 6);
  const int r = lane & 15, q = lane >> 4;
  const int bid = blockIdx.x;
  if (bid >= 448) {
    ST(1, 0);
    const int xcd = bid & 7, idx = (bid - 448) >> 3;
    const int mt = 4 * xcd + (idx & 3), nt = idx >> 2;
    const int rot0 = 3 * wid + (idx & 3);
    const __amdgpu_buffer_rsrc_t rs = __builtin_amdgcn_make_buffer_rsrc(ws, 0, WS_XT, 0x00020000);
    const int hA = WS_H + ((4 * mt) * 56 + 14 * wid) * 1024;
    const int wB = WS_WD + ((14 * wid) * 28 + 4 * nt) * 1024;
    const unsigned lo16 = lane * 16;
    f32x4 acc[4][4];
    u32x4 a[3][4], b[3][4];
    auto load = [&](int buf, int s) {
      s += rot0; s = s >= 14 ? s - 14 : s;
#pragma unroll
      for (int i = 0; i < 4; ++i) {
        a[buf][i] = __builtin_amdgcn_raw_buffer_load_b128(rs, lo16, hA + (i * 56 + s) * 1024, 0);
        b[buf][i] = __builtin_amdgcn_raw_buffer_load_b128(rs, lo16, wB + (s * 28 + ((i + wid) & 3)) * 1024, 0);
      }
    };
    auto comp = [&](int buf) {
#pragma unroll
      for (int j = 0; j < 4; ++j)
#pragma unroll
        for (int i = 0; i < 4; ++i)
          acc[j][i] = __builtin_amdgcn_mfma_f32_16x16x32_bf16(__builtin_bit_cast(bf16x8, b[buf][j]),
                                                              __builtin_bit_cast(bf16x8, a[buf][i]), acc[j][i], 0, 0, 0);
    };
    __builtin_amdgcn_s_sleep(8);
    load(0, 0); load(1, 1); load(2, 2);
#pragma unroll
    for (int i = 0; i < 4; ++i)
#pragma unroll
      for (int j = 0; j < 4; ++j) acc[i][j] = (f32x4){0, 0, 0, 0};
#pragma unroll 1
    for (int it = 0; it < 9; it += 3) {
      comp(0); load(0, it + 3);
      comp(1); load(1, it + 4);
      comp(2); load(2, it + 5);
    }
    comp(0); load(0, 12);
    comp(1); load(1, 13);
    comp(2); comp(0); comp(1);
    ST(1, 1);
#pragma unroll
    for (int j = 1; j < 4; ++j) {
      char* dp = lds + ((((j + wid) & 3) * 3 + (j - 1)) * 4) * 1024 + lo16;
#pragma unroll
      for (int i = 0; i < 4; ++i) *(f32x4*)(dp + i * 1024) = acc[j][i];
    }
    __builtin_amdgcn_sched_barrier(0);
    u32x2 xv[4];
#pragma unroll
    for (int i = 0; i < 4; ++i) xv[i] = *(const u32x2*)(x + ((size_t)(64 * mt + 16 * i + r) * DM + 64 * nt + 16 * wid + 4 * q) * 2);
    if (wid == 0) {
      GAS unsigned* cnt = (GAS unsigned*)(ws + WS_CNT + nt * 256);
      for (unsigned spins = 0; spins < (1u << 22); ++spins) {
        if ((unsigned)__builtin_amdgcn_readfirstlane(__hip_atomic_load(cnt, RLX_AGENT)) == 64u) break;
        __builtin_amdgcn_s_sleep(16);
      }
    }
    __syncthreads();
    const __amdgpu_buffer_rsrc_t ry = __builtin_amdgcn_make_buffer_rsrc(ws + WS_YT, 0, 1835008, 0x00020000);
    u32x4 yv4[2];
#pragma unroll
    for (int i = 0; i < 2; ++i) {
      const int c = tid + 256 * i;
      yv4[i] = __builtin_amdgcn_raw_buffer_load_b128(ry, ((64 * nt + (c >> 3)) * SEQ + 64 * mt) * 2 + (c & 7) * 16, 0, 16);
    }
#pragma unroll
    for (int sl = 0; sl < 3; ++sl)
#pragma unroll
      for (int i = 0; i < 4; ++i) acc[0][i] += *(const f32x4*)(lds + ((wid * 3 + sl) * 4 + i) * 1024 + lo16);
    __syncthreads();
#pragma unroll
    for (int i = 0; i < 2; ++i) {
      const int c = tid + 256 * i;
      *(u32x4*)(lds + (c >> 3) * 144 + (c & 7) * 16) = yv4[i];
    }
    __syncthreads();
    {
      const int qq = (lane & 15) >> 2, p = lane & 3;
#pragma unroll
      for (int i = 0; i < 4; ++i) {
        const u32x2 yv = tr_read(lds + (16 * wid + 4 * q + qq) * 144 + (16 * i + 4 * p) * 2);
        const unsigned b01 = pk_bf16o(acc[0][i][0], acc[0][i][1]), b23 = pk_bf16o(acc[0][i][2], acc[0][i][3]);
        const unsigned z01 = pk_bf16o(bf_lo(xv[i][0]) + bf_lo(yv[0]), bf_hi(xv[i][0]) + bf_hi(yv[0]));
        const unsigned z23 = pk_bf16o(bf_lo(xv[i][1]) + bf_lo(yv[1]), bf_hi(xv[i][1]) + bf_hi(yv[1]));
        u32x2 o = {pk_bf16(bf_lo(z01) + bf_lo(b01), bf_hi(z01) + bf_hi(b01)), pk_bf16(bf_lo(z23) + bf_lo(b23), bf_hi(z23) + bf_hi(b23))};
        *(u32x2*)(out + ((size_t)(64 * mt + 16 * i + r) * DM + 64 * nt + 16 * wid + 4 * q) * 2) = o;
      }
    }
    ST(1, 2);
    return;
  }
  const int d = bid;
  __builtin_amdgcn_s_setprio(2);
  ST(1, 0);
  const char* xt = ws + WS_XT + (size_t)d * SEQ * 2;
  const u32x4 xa = *(const u32x4*)(xt + tid * 16);
  const u32x2 xb = *(const u32x2*)(xt + (tid < 255 ? tid * 16 + 16 : 0));
  const u32x4 kr = *(const u32x4*)(ws + WS_KR + (size_t)d * 4096 + tid * 16);
  const float kun = *(const float*)(ws + WS_KS + d * 4);
  const float dsk = bf1(Dskip, d);
  if (tid >= 224) *(u32x4*)(lds + CV_KR + 4096 + (tid - 224) * 16) = (u32x4){0, 0, 0, 0};
  {
    char* xc = lds + tid * 16 + 32;
    const unsigned s0 = __builtin_amdgcn_alignbyte(xa[1], xa[0], 2), s1 = __builtin_amdgcn_alignbyte(xa[2], xa[1], 2),
                   s2 = __builtin_amdgcn_alignbyte(xa[3], xa[2], 2), s3 = __builtin_amdgcn_alignbyte(xb[0], xa[3], 2),
                   s4 = __builtin_amdgcn_alignbyte(xb[1], xb[0], 2);
    *(u32x4*)(xc) = xa;
    *(u32x4*)(xc + CV_XS) = (u32x4){s0, s1, s2, s3};
    *(u32x4*)(xc + 2 * CV_XS) = (u32x4){xa[1], xa[2], xa[3], xb[0]};
    *(u32x4*)(xc + 3 * CV_XS) = (u32x4){s1, s2, s3, s4};
    if (tid == 0) {
      u32x4 z = {0, 0, 0, 0};
      *(u32x4*)(lds) = z; *(u32x4*)(lds + 16) = z;
      *(u32x4*)(lds + CV_XS) = z; *(u32x4*)(lds + CV_XS + 16) = (u32x4){0, 0, 0, xa[0] << 16};
      *(u32x4*)(lds + 2 * CV_XS) = z; *(u32x4*)(lds + 2 * CV_XS + 16) = (u32x4){0, 0, 0, xa[0]};
      *(u32x4*)(lds + 3 * CV_XS) = z; *(u32x4*)(lds + 3 * CV_XS + 16) = (u32x4){0, 0, xa[0] << 16, s0};
    }
    *(u32x4*)(lds + CV_KR + tid * 16) = kr;
  }
  __syncthreads();
  ST(1, 4);
  f32x4 acc[8];
  {
    const int e0 = 8 * q + r + 1;
    const char* xp = lds + (e0 & 3) * CV_XS + (e0 & ~3) * 2 + wid * 128;
    const char* kp = lds + CV_KR + (2032 + 64 * wid - 16 * r + 8 * q) * 2;
#pragma unroll
    for (int rr = 0; rr < 2; ++rr) {
      f16x8 G[8];
#pragma unroll
      for (int dl = 0; dl < 8; ++dl) G[dl] = *(const f16x8*)(kp + rr * 64 - dl * 512);
#pragma unroll
      for (int i = 0; i < 8; ++i) {
        const char* fp = xp + (rr + 8 * i) * 64;
        u32x2 f0 = *(const u32x2*)fp, f1 = *(const u32x2*)(fp + 8);
        f16x8 Fr = __builtin_bit_cast(f16x8, (u32x4){f0[0], f0[1], f1[0], f1[1]});
#pragma unroll
        for (int nn = i; nn < 8; ++nn)
          acc[nn] = __builtin_amdgcn_mfma_f32_16x16x32_f16(Fr, G[nn - i], (rr == 0 && i == 0) ? (f32x4){0, 0, 0, 0} : acc[nn], 0, 0, 0);
      }
    }
  }
  ST(1, 5);
  u32x2 xo[2];
#pragma unroll
  for (int i = 0; i < 2; ++i) xo[i] = *(const u32x2*)(lds + (256 * (2 * wid + i) + 16 * r + 4 * q + 16) * 2);
  __syncthreads();
#pragma unroll
  for (int j = 0; j < 8; ++j) *(f32x4*)(lds + (j * 4 + wid) * 1024 + lane * 16) = acc[j];
  __syncthreads();
  u32x2 yv[2];
#pragma unroll
  for (int i = 0; i < 2; ++i) {
    const char* rp = lds + (2 * wid + i) * 4096 + lane * 16;
    f32x4 o = ((*(const f32x4*)rp + *(const f32x4*)(rp + 1024)) + (*(const f32x4*)(rp + 2048) + *(const f32x4*)(rp + 3072))) * kun;
    const u32x2 xoi = xo[i];
    f16x4 xv = __builtin_bit_cast(f16x4, xoi);
    const unsigned c01 = pk_bf16o(o[0], o[1]), c23 = pk_bf16o(o[2], o[3]);
    const unsigned s01 = pk_bf16o((float)xv[0] * dsk, (float)xv[1] * dsk), s23 = pk_bf16o((float)xv[2] * dsk, (float)xv[3] * dsk);
    yv[i] = (u32x2){pk_bf16(bf_lo(c01) + bf_lo(s01), bf_hi(c01) + bf_hi(s01)), pk_bf16(bf_lo(c23) + bf_lo(s23), bf_hi(c23) + bf_hi(s23))};
  }
  {
    auto e0 = __builtin_amdgcn_permlane16_swap(yv[0][0], yv[1][0], false, false);
    auto e1 = __builtin_amdgcn_permlane16_swap(yv[0][1], yv[1][1], false, false);
    const __amdgpu_buffer_rsrc_t ry = __builtin_amdgcn_make_buffer_rsrc(ws + WS_YT, 0, 1835008, 0x00020000);
    __builtin_amdgcn_raw_buffer_store_b128((u32x4){e0[0], e1[0], e0[1], e1[1]}, ry,
                                           (d * SEQ + 256 * (2 * wid + (q & 1)) + 16 * r + 8 * (q >> 1)) * 2, 0, 16);
  }
  asm volatile("s_waitcnt vmcnt(0)" ::: "memory");
  __syncthreads();
  if (tid == 0) __hip_atomic_fetch_add((GAS unsigned*)(ws + WS_CNT + (d >> 6) * 256), 1u, RLX_AGENT);
  ST(1, 6);
}

extern "C" void kernel_launch(void* const* d_in, const int* in_sizes, int n_in, void* d_out, int out_size, void* d_ws,
                              size_t ws_size, hipStream_t stream) {
  const char* x = (const char*)d_in[0];
  char* ws = (char*)d_ws;
  (void)hipFuncSetAttribute((const void*)k1_kernel, hipFuncAttributeMaxDynamicSharedMemorySize, K1_LDS);
  (void)hipFuncSetAttribute((const void*)k2_kernel, hipFuncAttributeMaxDynamicSharedMemorySize, K2_LDS);
  k0_kernel<<<448 + 128 + 56, 256, K0_LDS, stream>>>(x, (const char*)d_in[1], (const char*)d_in[2], (const char*)d_in[3],
                                                     (const char*)d_in[4], (const char*)d_in[5], (const char*)d_in[6],
                                                     (const char*)d_in[7], (const char*)d_in[12], ws);
  k1_kernel<<<224, 768, K1_LDS, stream>>>((const char*)d_in[10], (const char*)d_in[11], ws);
  k2_kernel<<<448 + 224, 256, K2_LDS, stream>>>((const char*)d_in[8], x, (char*)d_out, ws);
}
```

```cpp
#include <hip/hip_runtime.h>
#include <hip/hip_bf16.h>
#include <stdint.h>

typedef __attribute__((ext_vector_type(8))) __bf16 bf16x8;
typedef __attribute__((ext_vector_type(2))) __bf16 bf16x2;
typedef __attribute__((ext_vector_type(8))) _Float16 f16x8;
typedef __attribute__((ext_vector_type(4))) _Float16 f16x4;
typedef __attribute__((ext_vector_type(2))) _Float16 f16x2;
typedef __attribute__((ext_vector_type(4))) float f32x4;
typedef __attribute__((ext_vector_type(2))) float f32x2;
typedef __attribute__((ext_vector_type(4))) unsigned u32x4;
typedef __attribute__((ext_vector_type(2))) unsigned u32x2;
typedef __attribute__((ext_vector_type(4))) short s16x4;
#define LAS __attribute__((address_space(3)))
#define GAS __attribute__((address_space(1)))
#define RLX_AGENT __ATOMIC_RELAXED, __HIP_MEMORY_SCOPE_AGENT

#define SEQ 2048
#define DM 448
#define NS 256
#define FF 1792

#define WS_H 0u
#define WS_WD 7340032u
#define WS_XT (WS_WD + 1605632u)
#define WS_YT (WS_XT + 1835008u)
#define WS_XN (WS_YT + 1835008u)
#define WS_KR (WS_XN + 1835008u)
#define WS_KS (WS_KR + 1835008u)
#define WS_CNT (WS_KS + 2048u)

__device__ __forceinline__ unsigned pk_bf16(float a, float b) {
  bf16x2 v = {(__bf16)a, (__bf16)b};
  return __builtin_bit_cast(unsigned, v);
}
__device__ __forceinline__ unsigned pk_bf16o(float a, float b) {
  unsigned r;
  asm("v_cvt_pk_bf16_f32 %0, %1, %2" : "=v"(r) : "v"(a), "v"(b));
  return r;
}
__device__ __forceinline__ unsigned pk_f16(float a, float b) {
  f16x2 v = {(_Float16)a, (_Float16)b};
  return __builtin_bit_cast(unsigned, v);
}
__device__ __forceinline__ float bf_lo(unsigned u) { return __uint_as_float(u << 16); }
__device__ __forceinline__ float bf_hi(unsigned u) { return __uint_as_float(u & 0xffff0000u); }
__device__ __forceinline__ float rbf(float x) { return (float)(__bf16)x; }
__device__ __forceinline__ float bf1(const void* p, int i) {
  return __uint_as_float(((unsigned)((const unsigned short*)p)[i]) << 16);
}
__device__ __forceinline__ u32x2 tr_read(const char* lds) {
  s16x4 v = __builtin_amdgcn_ds_read_tr16_b64_v4i16((LAS s16x4*)lds);
  return __builtin_bit_cast(u32x2, v);
}
#define ST(kid, i)
#define WAIT_V(n) asm volatile("s_waitcnt vmcnt(" #n ")" ::: "memory")
#define BAR() asm volatile("s_waitcnt lgkmcnt(0)\n\ts_barrier" ::: "memory")
__device__ __forceinline__ void glds16(const char* sbase, unsigned voff, unsigned lds_dst) {
  unsigned keep;
  asm volatile("s_mov_b32 %0, m0\n\ts_mov_b32 m0, %3\n\ts_nop 0\n\tglobal_load_lds_dwordx4 %1, %2\n\ts_mov_b32 m0, %0"
               : "=&s"(keep) : "v"(voff), "s"(sbase), "s"(lds_dst) : "memory");
}
__device__ __forceinline__ float sum_q(float v) {
  unsigned u = __float_as_uint(v);
  auto a = __builtin_amdgcn_permlane16_swap(u, u, false, false);
  float s = __uint_as_float(a[0]) + __uint_as_float(a[1]);
  u = __float_as_uint(s);
  auto b = __builtin_amdgcn_permlane32_swap(u, u, false, false);
  return __uint_as_float(b[0]) + __uint_as_float(b[1]);
}

#define KG_TW 4608
#define KG_KC (4 * KG_TW)
#define KG_MISC (KG_KC + 1536)
#define K0_LDS 28672

__device__ __forceinline__ f32x2 cmul2(f32x2 a, f32x2 b, f32x2 bs) {
  f32x2 t = a.xx * b;
  return __builtin_elementwise_fma(a.yy, bs, t);
}
__device__ __forceinline__ f32x2 rot(f32x2 b) { return (f32x2){-b.y, b.x}; }
__device__ __forceinline__ f32x2 csq(f32x2 a) {
  float r = fmaf(a.x, a.x, -(a.y * a.y)), i = (a.x + a.x) * a.y;
  asm volatile("" : "+v"(r), "+v"(i));
  return (f32x2){r, i};
}
__device__ __forceinline__ f32x2 cexp_tau(float re_l2, float rev_hi, float rev_lo, float tau) {
  float mag = __builtin_amdgcn_exp2f(re_l2 * tau);
  float fr = __builtin_amdgcn_fractf(rev_hi * tau) + rev_lo * tau;
  return (f32x2){mag * __builtin_amdgcn_cosf(fr), mag * __builtin_amdgcn_sinf(fr)};
}
#define DPP(v, ctrl) __int_as_float(__builtin_amdgcn_update_dpp(0, __float_as_int(v), ctrl, 0xf, 0xf, false))

__global__ __launch_bounds__(256) void k0_kernel(const char* __restrict__ x, const char* __restrict__ Are, const char* __restrict__ Aim,
                                                 const char* __restrict__ Bre, const char* __restrict__ Bim,
                                                 const char* __restrict__ Cre, const char* __restrict__ Cim,
                                                 const char* __restrict__ logdt, const char* __restrict__ wd,
                                                 const char* __restrict__ wg, const char* __restrict__ wu, char* __restrict__ ws) {
  extern __shared__ __attribute__((aligned(16))) char lds[];
  const int tid = threadIdx.x, lane = tid & 63;
  const int wid = __builtin_amdgcn_readfirstlane(tid >> 6);
  const int r = lane & 15, q = lane >> 4, qq = (lane & 15) >> 2, p = lane & 3;
  const int bid = blockIdx.x;
  if (bid >= 632) {
    const int xcd = bid & 7, part = (bid - 632) >> 3;
    const char* base = (tid < 128 ? wg : wu) + (size_t)(56 * part) * (FF * 2) + (xcd >> 2) * 1792 + (tid & 127) * 16;
    u32x4 acc = {0, 0, 0, 0};
    if ((tid & 127) < 112) {
#pragma unroll 8
      for (int i = 0; i < 56; ++i) acc ^= *(const u32x4*)(base + (size_t)i * (FF * 2));
    }
    asm volatile("" :: "v"(acc));
    return;
  }
  if (bid >= 576) {
    if (bid == 576 && tid < 7) *(unsigned*)(ws + WS_CNT + tid * 256) = 0u;
    const unsigned lbase = (unsigned)(size_t)(LAS char*)lds;
    const int u = (bid - 576) * 4 + wid, s2 = u >> 2, cq = u & 3;
    const char* src = wd + (size_t)(32 * s2) * (DM * 2) + cq * 224;
#pragma unroll
    for (int i = 0; i < 7; ++i) glds16(src + i * 32, (lane >> 1) * (DM * 2) + (lane & 1) * 16, lbase + wid * 7168 + i * 1024);
    WAIT_V(0);
    const char* bp = lds + wid * 7168 + (4 * q + qq) * 32 + p * 8;
#pragma unroll
    for (int i = 0; i < 7; ++i) {
      u32x2 lo = tr_read(bp + i * 1024), hi = tr_read(bp + i * 1024 + 512);
      *(u32x4*)(ws + WS_WD + (size_t)(s2 * 28 + 7 * cq + i) * 1024 + lane * 16) = (u32x4){lo[0], lo[1], hi[0], hi[1]};
    }
    return;
  }
  if (bid >= 448) {
    const int tok0 = (bid - 448) * 16;
    const size_t rowo = (size_t)(tok0 + r) * (DM * 2) + q * 16 + wid * 64;
    u32x4 xr[4];
#pragma unroll
    for (int i = 0; i < 3; ++i) xr[i] = *(const u32x4*)(x + rowo + i * 256);
    xr[3] = (u32x4){0, 0, 0, 0};
    if (wid < 2) xr[3] = *(const u32x4*)(x + rowo + 768);
    float s1 = 0, s2 = 0;
#pragma unroll
    for (int i = 0; i < 4; ++i)
#pragma unroll
      for (int w = 0; w < 4; ++w) {
        const unsigned v = xr[i][w];
        const float lo = bf_lo(v), hi = bf_hi(v);
        s1 += lo + hi;
        s2 = fmaf(lo, lo, fmaf(hi, hi, s2));
      }
    s1 = sum_q(s1); s2 = sum_q(s2);
    if (q == 0) *(f32x2*)(lds + (wid * 16 + r) * 8) = (f32x2){s1, s2};
    __syncthreads();
    float t1 = 0, t2 = 0;
#pragma unroll
    for (int w = 0; w < 4; ++w) { const f32x2 v = *(const f32x2*)(lds + (w * 16 + r) * 8); t1 += v.x; t2 += v.y; }
    const float mean = t1 * (1.0f / DM);
    const float var = fmaxf(t2 * (1.0f / DM) - mean * mean, 0.0f);
    const float mb = rbf(mean);
    const float rs = 1.0f / rbf(sqrtf(rbf(rbf(var) + 1e-5f)));
    char* XT = lds + 1024 + wid * 4096;
#pragma unroll
    for (int i = 0; i < 4; ++i) {
      if (i < 3 || wid < 2) {
        u32x4 o;
#pragma unroll
        for (int w = 0; w < 4; ++w) {
          const unsigned v = xr[i][w];
          const unsigned d = pk_bf16o(bf_lo(v) - mb, bf_hi(v) - mb);
          o[w] = pk_bf16o(bf_lo(d) * rs, bf_hi(d) * rs);
        }
        *(u32x4*)(ws + WS_XN + rowo + i * 256) = o;
        *(u32x4*)(XT + i * 1024 + lane * 16) = o;
      }
    }
    __builtin_amdgcn_wave_barrier();
#pragma unroll
    for (int i = 0; i < 4; ++i) {
      if (i < 3 || wid < 2) {
#pragma unroll
        for (int cb = 0; cb < 2; ++cb) {
          u32x2 v = tr_read(XT + i * 1024 + ((2 * cb + (p >> 1)) * 16 + 4 * q + qq) * 16 + 8 * (p & 1));
          u32x2 o = {pk_f16(bf_lo(v[0]), bf_hi(v[0])), pk_f16(bf_lo(v[1]), bf_hi(v[1]))};
          *(u32x2*)(ws + WS_XT + ((size_t)(32 * (wid + 4 * i) + 16 * cb + r) * SEQ + tok0 + 4 * q) * 2) = o;
        }
      }
    }
    return;
  }
  const int d = bid;
  ST(2, 0);
  const int n = d * NS + tid;
  const float a_re = bf1(Are, n), a_im = bf1(Aim, n), b_re = bf1(Bre, n), b_im = bf1(Bim, n), cr = bf1(Cre, n), ci = bf1(Cim, n);
  const float ldt = bf1(logdt, d);
  u32x4 kidx;
#pragma unroll
  for (int w = 0; w < 4; ++w) {
    unsigned e[2];
#pragma unroll
    for (int h = 0; h < 2; ++h) {
      const int lag = 2047 - 8 * tid - 2 * w - h;
      const int lb = (int)rbf((float)lag);
      const int seg = (31 - __builtin_clz(lb | 128)) - 7;
      const int o = lb & ~(seg ? (128 << seg) : 0);
      const int row = (seg ? 8 + 8 * seg : 0) + ((o & 7) >> seg) + ((o >> 7) << 3 >> seg);
      e[h] = (row * 16 + ((lb >> 3) & 15)) * 2;
    }
    kidx[w] = e[0] | (e[1] << 16);
  }
  asm volatile("" : "+v"(kidx));
  {
    const float dt = rbf(__expf(ldt));
    const float re = dt * a_re, im = dt * a_im, br = dt * b_re, bi = dt * b_im;
    const float CBr = cr * br - ci * bi, CBi = cr * bi + ci * br;
    const float re_l2 = re * 1.44269504088896340736f;
    const float rev_hi = im * 0.15915494309189535f;
    const float rev_lo = fmaf(im, 0.15915494309189535f, -rev_hi) + im * 6.4206e-9f;
    const float sig = fmaxf(1.0f, __builtin_amdgcn_exp2f(re_l2 * 120.0f));
    const float gmx = fmaxf(1.0f, __builtin_amdgcn_exp2f(re_l2 * 2048.0f));
    const float cb1 = (fabsf(CBr) + fabsf(CBi)) * gmx;
    float mx = cb1 * sig, sm = cb1;
    mx = fmaxf(mx, DPP(mx, 0x111)); sm += DPP(sm, 0x111);
    mx = fmaxf(mx, DPP(mx, 0x112)); sm += DPP(sm, 0x112);
    mx = fmaxf(mx, DPP(mx, 0x114)); sm += DPP(sm, 0x114);
    mx = fmaxf(mx, DPP(mx, 0x118)); sm += DPP(sm, 0x118);
    mx = fmaxf(mx, DPP(mx, 0x142)); sm += DPP(sm, 0x142);
    mx = fmaxf(mx, DPP(mx, 0x143)); sm += DPP(sm, 0x143);
    if (lane == 63) *(float*)(lds + KG_MISC + wid * 4) = sm;
    mx = __int_as_float(__builtin_amdgcn_readlane(__float_as_int(mx), 63));
    const int ew = __builtin_amdgcn_frexp_expf(mx);
    const float scw = __builtin_amdgcn_ldexpf(sig, 15 - ew);
    const float unsc = __builtin_amdgcn_ldexpf(1.0f, ew - 30);
    const f32x2 z1 = cexp_tau(re_l2, rev_hi, rev_lo, 1.0f);
    const f32x2 z8 = cexp_tau(re_l2, rev_hi, rev_lo, 8.0f);
    const f32x2 z128 = cexp_tau(re_l2, rev_hi, rev_lo, 128.0f);
    ST(2, 1);
    char* T = lds + wid * KG_TW;
    char* Tw = T + lane * 4;
    const char* Tr = T + r * 288 + q * 16;
    auto put = [&](int row, f32x2 g) { *(unsigned*)(Tw + row * 288) = pk_f16(g.x, g.y); };
    {
      const f32x2 z8s = rot(z8);
      f32x2 e = {32768.0f * __builtin_amdgcn_rcpf(sig), 0.0f};
#pragma unroll
      for (int j = 0; j < 16; ++j) {
        put(j, e);
        if (j < 15) e = cmul2(e, z8, z8s);
      }
    }
    __builtin_amdgcn_wave_barrier();
    f16x8 E[4];
#pragma unroll
    for (int s = 0; s < 4; ++s) E[s] = *(const f16x8*)(Tr + s * 64);
    __builtin_amdgcn_wave_barrier();
    const f32x2 y1 = {z1.x, -z1.y}, y128 = {z128.x, -z128.y};
    const f32x2 y1s = rot(y1), y128s = rot(y128);
    const f32x2 H0 = {CBr * scw, -CBi * scw};
    const f32x2 y2 = csq(y1), y4 = csq(y2), y256 = csq(y128), y512 = csq(y256), y1024 = csq(y512);
    f32x4 kacc[3];
    auto mm = [&](int t) {
      __builtin_amdgcn_wave_barrier();
      f32x4 a = {0, 0, 0, 0};
#pragma unroll
      for (int s = 0; s < 4; ++s) {
        f16x8 g = *(const f16x8*)(Tr + s * 64);
        a = __builtin_amdgcn_mfma_f32_16x16x32_f16(E[s], g, a, 0, 0, 0);
      }
      kacc[t] = a;
      __builtin_amdgcn_wave_barrier();
    };
    {
      f32x2 g = H0;
#pragma unroll
      for (int i = 0; i < 8; ++i) {
        put(i, g); put(8 + i, cmul2(g, y128, y128s));
        if (i < 7) g = cmul2(g, y1, y1s);
      }
    }
    mm(0);
    {
      const f32x2 y2s = rot(y2);
      f32x2 g = cmul2(H0, y256, rot(y256));
#pragma unroll
      for (int i = 0; i < 4; ++i) {
        put(i, g); put(4 + i, cmul2(g, y128, y128s));
        if (i < 3) g = cmul2(g, y2, y2s);
      }
      f32x2 ga = cmul2(H0, y512, rot(y512)), gb = cmul2(ga, y4, rot(y4));
#pragma unroll
      for (int i = 0; i < 4; ++i) {
        put(8 + 2 * i, ga); put(9 + 2 * i, gb);
        if (i < 3) { ga = cmul2(ga, y128, y128s); gb = cmul2(gb, y128, y128s); }
      }
    }
    mm(1);
    {
      f32x2 g = cmul2(H0, y1024, rot(y1024));
#pragma unroll
      for (int i = 0; i < 9; ++i) {
        put(i, g);
        if (i < 8) g = cmul2(g, y128, y128s);
      }
    }
    mm(2);
#pragma unroll
    for (int t = 0; t < 3; ++t) *(f32x4*)(T + ((16 * t + r) * 16 + 4 * q) * 4) = kacc[t] * unsc;
  }
  ST(2, 2);
  __syncthreads();
  {
    const f32x4 ms = *(const f32x4*)(lds + KG_MISC);
    const int ek = __builtin_amdgcn_frexp_expf((ms[0] + ms[1]) + (ms[2] + ms[3]));
    if (tid == 255) *(float*)(ws + WS_KS + d * 4) = __builtin_amdgcn_ldexpf(1.0f, ek - 15);
    if (tid < 192) {
      const float ksc = __builtin_amdgcn_ldexpf(1.0f, 15 - ek);
      const char* kp = lds + tid * 16;
      f32x4 v = ((*(const f32x4*)kp + *(const f32x4*)(kp + KG_TW)) + (*(const f32x4*)(kp + 2 * KG_TW) + *(const f32x4*)(kp + 3 * KG_TW))) * ksc;
      *(u32x2*)(lds + KG_KC + tid * 8) = (u32x2){pk_f16(v[0], v[1]), pk_f16(v[2], v[3])};
    }
  }
  __syncthreads();
  {
    const char* kc = lds + KG_KC;
    u32x4 o;
#pragma unroll
    for (int w = 0; w < 4; ++w) {
      const unsigned iv = kidx[w];
      f16x2 h;
      h.x = *(const _Float16*)(kc + (iv & 0xffffu));
      h.y = *(const _Float16*)(kc + (iv >> 16));
      o[w] = __builtin_bit_cast(unsigned, h);
    }
    *(u32x4*)(ws + WS_KR + (size_t)d * 4096 + tid * 16) = o;
  }
  ST(2, 3);
}

#define K1_SLOT 49152
#define K1_LDS (3 * K1_SLOT)

__global__ __launch_bounds__(768, 3) void k1_kernel(const char* __restrict__ wg, const char* __restrict__ wu, char* __restrict__ ws) {
  extern __shared__ __attribute__((aligned(16))) char lds[];
  const int tid = threadIdx.x, lane = tid & 63;
  const int wid = __builtin_amdgcn_readfirstlane(tid >> 6);
  const int bid = blockIdx.x;
  const unsigned lbase = (unsigned)(size_t)(LAS char*)lds;
  const int r = lane & 15, q = lane >> 4, qq = (lane & 15) >> 2, p = lane & 3;
  const int xcd = bid & 7, idx = bid >> 3;
  const int mt = 4 * (xcd & 3) + (idx & 3);
  const int jt = 7 * (xcd >> 2) + (idx >> 2);
  const int m = (wid >> 2) & 1, n = wid & 3;
  ST(0, 0);
  if (wid >= 8) {
    const int pw = wid - 8;
    const unsigned aoff = (lane >> 3) * (DM * 2) + (((lane & 7) ^ (lane >> 3)) * 16);
    const char* xsrc = ws + WS_XN + (size_t)(mt * 128 + pw * 16) * (DM * 2);
    const unsigned boff = (8 * pw + q) * (FF * 2) + ((((r >> 1) ^ ((4 * (pw & 1) + q) & 7)) * 2 + (r & 1)) * 16);
    const char* gsrc = wg + jt * 256;
    const char* usrc = wu + jt * 256;
    auto dma_chunk = [&](int c, int slot) {
      const unsigned dst = lbase + slot * K1_SLOT + pw * 1024;
      const size_t wo = (size_t)c * (64 * FF * 2);
#pragma unroll
      for (int h = 0; h < 2; ++h) {
        glds16(xsrc + c * 128 + h * 64 * DM * 2, aoff, dst + pw * 1024 + h * 8192);
        glds16(xsrc + c * 128 + h * 64 * DM * 2 + 8 * DM * 2, aoff, dst + pw * 1024 + h * 8192 + 1024);
        glds16(gsrc + wo + h * 4 * FF * 2, boff, dst + 16384 + h * 4096);
        glds16(usrc + wo + h * 4 * FF * 2, boff, dst + 32768 + h * 4096);
        glds16(gsrc + wo + (32 + h * 4) * FF * 2, boff, dst + 16384 + 8192 + h * 4096);
        glds16(usrc + wo + (32 + h * 4) * FF * 2, boff, dst + 32768 + 8192 + h * 4096);
      }
    };
    dma_chunk(0, 0); dma_chunk(1, 1);
    WAIT_V(12);
    BAR();
    dma_chunk(2, 2);
    int slot = 0;
#pragma unroll 1
    for (int c = 0; c < 6; ++c) {
      if (c < 5) WAIT_V(12); else WAIT_V(0);
      BAR();
      if (c < 4) dma_chunk(c + 3, slot);
      slot = slot == 2 ? 0 : slot + 1;
    }
    return;
  }
  f32x4 acc[4][4];
#pragma unroll
  for (int i = 0; i < 4; ++i)
#pragma unroll
    for (int j = 0; j < 4; ++j) acc[i][j] = (f32x4){0, 0, 0, 0};
  const int x7 = (4 * q + qq) & 7;
  const int brd0 = 16384 + (4 * q + qq) * 256 + (((2 * n) ^ x7) * 32) + p * 8;
  const int brd1 = brd0 ^ 32;
  const int ard0 = (4 * m) * 2048 + (r >> 3) * 1024 + (r & 7) * 128 + ((q ^ (r & 7)) * 16);
  const int ard1 = ard0 ^ 64;
  u32x4 a[2][4], b[2][4];
  auto rdA = [&](int ks, const char* S, int i) { a[ks][i] = *(const u32x4*)(S + (ks ? ard1 : ard0) + i * 2048); };
  auto rdB = [&](int ks, const char* S, int t) {
    const char* bp = S + ((t & 1) ? brd1 : brd0) + (t >> 1) * 16384 + ks * 8192;
    u32x2 lo = tr_read(bp), hi = tr_read(bp + 4096);
    b[ks][t] = (u32x4){lo[0], lo[1], hi[0], hi[1]};
  };
  auto mm = [&](int ks, const char* S) {
#pragma unroll
    for (int t = 0; t < 4; ++t) {
#pragma unroll
      for (int i = 0; i < 4; ++i) {
        acc[t][i] = __builtin_amdgcn_mfma_f32_16x16x32_bf16(__builtin_bit_cast(bf16x8, b[ks][t]), __builtin_bit_cast(bf16x8, a[ks][i]), acc[t][i], 0, 0, 0);
        const int st = 4 * t + i;
        if (st < 8) { if (st & 1) rdB(1 - ks, S, st >> 1); else rdA(1 - ks, S, st >> 1); }
        __builtin_amdgcn_sched_barrier(0);
      }
    }
    __builtin_amdgcn_s_waitcnt(0xC07F);
  };
  BAR();
  ST(0, 2);
#pragma unroll
  for (int t = 0; t < 4; ++t) { rdA(0, lds, t); rdB(0, lds, t); }
  __builtin_amdgcn_s_waitcnt(0xC07F);
  int slot = 0;
#pragma unroll 1
  for (int c = 0; c < 7; ++c) {
    mm(0, lds + slot * K1_SLOT);
    if (c < 6) {
      BAR();
      ST(0, 3 + c);
      slot = slot == 2 ? 0 : slot + 1;
    }
    mm(1, lds + slot * K1_SLOT);
  }
  ST(0, 9);
#pragma unroll
  for (int i = 0; i < 4; ++i) {
    u32x4 hv;
#pragma unroll
    for (int t = 0; t < 2; ++t) {
      const f32x4 g = acc[t][i], u = acc[t + 2][i];
      unsigned g01 = pk_bf16o(g[0], g[1]), g23 = pk_bf16o(g[2], g[3]);
      unsigned u01 = pk_bf16o(u[0], u[1]), u23 = pk_bf16o(u[2], u[3]);
      hv[2 * t] = pk_bf16(bf_lo(g01) * bf_lo(u01), bf_hi(g01) * bf_hi(u01));
      hv[2 * t + 1] = pk_bf16(bf_lo(g23) * bf_lo(u23), bf_hi(g23) * bf_hi(u23));
    }
    *(u32x4*)(ws + WS_H + ((size_t)(mt * 8 + 4 * m + i) * 56 + 4 * jt + n) * 1024 + lane * 16) = hv;
  }
  ST(0, 10);
}

#define K2_LDS 49152
#define CV_XS 4160
#define CV_KR 32768

__global__ __launch_bounds__(256, 3) void k2_kernel(const char* __restrict__ Dskip, const char* __restrict__ x, char* __restrict__ out, char* ws) {
  extern __shared__ __attribute__((aligned(16))) char lds[];
  const int tid = threadIdx.x, lane = tid & 63;
  const int wid = __builtin_amdgcn_readfirstlane(tid >> 6);
  const int r = lane & 15, q = lane >> 4;
  const int bid = blockIdx.x;
  if (bid >= 448) {
    ST(1, 0);
    const int xcd = bid & 7, idx = (bid - 448) >> 3;
    const int mt = 8 * (xcd & 3) + 4 * (xcd >> 2) + (idx & 3), nt = idx >> 2;
    const int rot0 = 3 * wid + (idx & 3);
    const __amdgpu_buffer_rsrc_t rs = __builtin_amdgcn_make_buffer_rsrc(ws, 0, WS_XT, 0x00020000);
    const int hA = WS_H + ((4 * mt) * 56 + 14 * wid) * 1024;
    const int wB = WS_WD + ((14 * wid) * 28 + 4 * nt) * 1024;
    const unsigned lo16 = lane * 16;
    f32x4 acc[4][4];
    u32x4 a[3][4], b[3][4];
    auto load = [&](int buf, int s) {
      s += rot0; s = s >= 14 ? s - 14 : s;
#pragma unroll
      for (int i = 0; i < 4; ++i) {
        a[buf][i] = __builtin_amdgcn_raw_buffer_load_b128(rs, lo16, hA + (i * 56 + s) * 1024, 0);
        b[buf][i] = __builtin_amdgcn_raw_buffer_load_b128(rs, lo16, wB + (s * 28 + ((i + wid) & 3)) * 1024, 0);
      }
    };
    auto comp = [&](int buf) {
#pragma unroll
      for (int j = 0; j < 4; ++j)
#pragma unroll
        for (int i = 0; i < 4; ++i)
          acc[j][i] = __builtin_amdgcn_mfma_f32_16x16x32_bf16(__builtin_bit_cast(bf16x8, b[buf][j]),
                                                              __builtin_bit_cast(bf16x8, a[buf][i]), acc[j][i], 0, 0, 0);
    };
    __builtin_amdgcn_s_sleep(8);
    load(0, 0); load(1, 1); load(2, 2);
#pragma unroll
    for (int i = 0; i < 4; ++i)
#pragma unroll
      for (int j = 0; j < 4; ++j) acc[i][j] = (f32x4){0, 0, 0, 0};
#pragma unroll 1
    for (int it = 0; it < 9; it += 3) {
      comp(0); load(0, it + 3);
      comp(1); load(1, it + 4);
      comp(2); load(2, it + 5);
    }
    comp(0); load(0, 12);
    comp(1); load(1, 13);
    comp(2); comp(0); comp(1);
    ST(1, 1);
#pragma unroll
    for (int j = 1; j < 4; ++j) {
      char* dp = lds + ((((j + wid) & 3) * 3 + (j - 1)) * 4) * 1024 + lo16;
#pragma unroll
      for (int i = 0; i < 4; ++i) *(f32x4*)(dp + i * 1024) = acc[j][i];
    }
    __builtin_amdgcn_sched_barrier(0);
    u32x2 xv[4];
#pragma unroll
    for (int i = 0; i < 4; ++i) xv[i] = *(const u32x2*)(x + ((size_t)(64 * mt + 16 * i + r) * DM + 64 * nt + 16 * wid + 4 * q) * 2);
    if (wid == 0) {
      GAS unsigned* cnt = (GAS unsigned*)(ws + WS_CNT + nt * 256);
      for (unsigned spins = 0; spins < (1u << 22); ++spins) {
        if ((unsigned)__builtin_amdgcn_readfirstlane(__hip_atomic_load(cnt, RLX_AGENT)) == 64u) break;
        __builtin_amdgcn_s_sleep(16);
      }
    }
    __syncthreads();
    const __amdgpu_buffer_rsrc_t ry = __builtin_amdgcn_make_buffer_rsrc(ws + WS_YT, 0, 1835008, 0x00020000);
    u32x4 yv4[2];
#pragma unroll
    for (int i = 0; i < 2; ++i) {
      const int c = tid + 256 * i;
      yv4[i] = __builtin_amdgcn_raw_buffer_load_b128(ry, ((64 * nt + (c >> 3)) * SEQ + 64 * mt) * 2 + (c & 7) * 16, 0, 16);
    }
#pragma unroll
    for (int sl = 0; sl < 3; ++sl)
#pragma unroll
      for (int i = 0; i < 4; ++i) acc[0][i] += *(const f32x4*)(lds + ((wid * 3 + sl) * 4 + i) * 1024 + lo16);
    __syncthreads();
#pragma unroll
    for (int i = 0; i < 2; ++i) {
      const int c = tid + 256 * i;
      *(u32x4*)(lds + (c >> 3) * 144 + (c & 7) * 16) = yv4[i];
    }
    __syncthreads();
    {
      const int qq = (lane & 15) >> 2, p = lane & 3;
#pragma unroll
      for (int i = 0; i < 4; ++i) {
        const u32x2 yv = tr_read(lds + (16 * wid + 4 * q + qq) * 144 + (16 * i + 4 * p) * 2);
        const unsigned b01 = pk_bf16o(acc[0][i][0], acc[0][i][1]), b23 = pk_bf16o(acc[0][i][2], acc[0][i][3]);
        const unsigned z01 = pk_bf16o(bf_lo(xv[i][0]) + bf_lo(yv[0]), bf_hi(xv[i][0]) + bf_hi(yv[0]));
        const unsigned z23 = pk_bf16o(bf_lo(xv[i][1]) + bf_lo(yv[1]), bf_hi(xv[i][1]) + bf_hi(yv[1]));
        u32x2 o = {pk_bf16(bf_lo(z01) + bf_lo(b01), bf_hi(z01) + bf_hi(b01)), pk_bf16(bf_lo(z23) + bf_lo(b23), bf_hi(z23) + bf_hi(b23))};
        *(u32x2*)(out + ((size_t)(64 * mt + 16 * i + r) * DM + 64 * nt + 16 * wid + 4 * q) * 2) = o;
      }
    }
    ST(1, 2);
    return;
  }
  const int d = bid;
  __builtin_amdgcn_s_setprio(2);
  ST(1, 0);
  const char* xt = ws + WS_XT + (size_t)d * SEQ * 2;
  const u32x4 xa = *(const u32x4*)(xt + tid * 16);
  const u32x2 xb = *(const u32x2*)(xt + (tid < 255 ? tid * 16 + 16 : 0));
  const u32x4 kr = *(const u32x4*)(ws + WS_KR + (size_t)d * 4096 + tid * 16);
  const float kun = *(const float*)(ws + WS_KS + d * 4);
  const float dsk = bf1(Dskip, d);
  if (tid >= 224) *(u32x4*)(lds + CV_KR + 4096 + (tid - 224) * 16) = (u32x4){0, 0, 0, 0};
  {
    char* xc = lds + tid * 16 + 32;
    const unsigned s0 = __builtin_amdgcn_alignbyte(xa[1], xa[0], 2), s1 = __builtin_amdgcn_alignbyte(xa[2], xa[1], 2),
                   s2 = __builtin_amdgcn_alignbyte(xa[3], xa[2], 2), s3 = __builtin_amdgcn_alignbyte(xb[0], xa[3], 2),
                   s4 = __builtin_amdgcn_alignbyte(xb[1], xb[0], 2);
    *(u32x4*)(xc) = xa;
    *(u32x4*)(xc + CV_XS) = (u32x4){s0, s1, s2, s3};
    *(u32x4*)(xc + 2 * CV_XS) = (u32x4){xa[1], xa[2], xa[3], xb[0]};
    *(u32x4*)(xc + 3 * CV_XS) = (u32x4){s1, s2, s3, s4};
    if (tid == 0) {
      u32x4 z = {0, 0, 0, 0};
      *(u32x4*)(lds) = z; *(u32x4*)(lds + 16) = z;
      *(u32x4*)(lds + CV_XS) = z; *(u32x4*)(lds + CV_XS + 16) = (u32x4){0, 0, 0, xa[0] << 16};
      *(u32x4*)(lds + 2 * CV_XS) = z; *(u32x4*)(lds + 2 * CV_XS + 16) = (u32x4){0, 0, 0, xa[0]};
      *(u32x4*)(lds + 3 * CV_XS) = z; *(u32x4*)(lds + 3 * CV_XS + 16) = (u32x4){0, 0, xa[0] << 16, s0};
    }
    *(u32x4*)(lds + CV_KR + tid * 16) = kr;
  }
  __syncthreads();
  ST(1, 4);
  f32x4 acc[8];
  {
    const int e0 = 8 * q + r + 1;
    const char* xp = lds + (e0 & 3) * CV_XS + (e0 & ~3) * 2 + wid * 128;
    const char* kp = lds + CV_KR + (2032 + 64 * wid - 16 * r + 8 * q) * 2;
#pragma unroll
    for (int rr = 0; rr < 2; ++rr) {
      f16x8 G[8];
#pragma unroll
      for (int dl = 0; dl < 8; ++dl) G[dl] = *(const f16x8*)(kp + rr * 64 - dl * 512);
#pragma unroll
      for (int i = 0; i < 8; ++i) {
        const char* fp = xp + (rr + 8 * i) * 64;
        u32x2 f0 = *(const u32x2*)fp, f1 = *(const u32x2*)(fp + 8);
        f16x8 Fr = __builtin_bit_cast(f16x8, (u32x4){f0[0], f0[1], f1[0], f1[1]});
#pragma unroll
        for (int nn = i; nn < 8; ++nn)
          acc[nn] = __builtin_amdgcn_mfma_f32_16x16x32_f16(Fr, G[nn - i], (rr == 0 && i == 0) ? (f32x4){0, 0, 0, 0} : acc[nn], 0, 0, 0);
      }
    }
  }
  ST(1, 5);
  u32x2 xo[2];
#pragma unroll
  for (int i = 0; i < 2; ++i) xo[i] = *(const u32x2*)(lds + (256 * (2 * wid + i) + 16 * r + 4 * q + 16) * 2);
  __syncthreads();
#pragma unroll
  for (int j = 0; j < 8; ++j) *(f32x4*)(lds + (j * 4 + wid) * 1024 + lane * 16) = acc[j];
  __syncthreads();
  u32x2 yv[2];
#pragma unroll
  for (int i = 0; i < 2; ++i) {
    const char* rp = lds + (2 * wid + i) * 4096 + lane * 16;
    f32x4 o = ((*(const f32x4*)rp + *(const f32x4*)(rp + 1024)) + (*(const f32x4*)(rp + 2048) + *(const f32x4*)(rp + 3072))) * kun;
    const u32x2 xoi = xo[i];
    f16x4 xv = __builtin_bit_cast(f16x4, xoi);
    const unsigned c01 = pk_bf16o(o[0], o[1]), c23 = pk_bf16o(o[2], o[3]);
    const unsigned s01 = pk_bf16o((float)xv[0] * dsk, (float)xv[1] * dsk), s23 = pk_bf16o((float)xv[2] * dsk, (float)xv[3] * dsk);
    yv[i] = (u32x2){pk_bf16(bf_lo(c01) + bf_lo(s01), bf_hi(c01) + bf_hi(s01)), pk_bf16(bf_lo(c23) + bf_lo(s23), bf_hi(c23) + bf_hi(s23))};
  }
  {
    auto e0 = __builtin_amdgcn_permlane16_swap(yv[0][0], yv[1][0], false, false);
    auto e1 = __builtin_amdgcn_permlane16_swap(yv[0][1], yv[1][1], false, false);
    const __amdgpu_buffer_rsrc_t ry = __builtin_amdgcn_make_buffer_rsrc(ws + WS_YT, 0, 1835008, 0x00020000);
    __builtin_amdgcn_raw_buffer_store_b128((u32x4){e0[0], e1[0], e0[1], e1[1]}, ry,
                                           (d * SEQ + 256 * (2 * wid + (q & 1)) + 16 * r + 8 * (q >> 1)) * 2, 0, 16);
  }
  asm volatile("s_waitcnt vmcnt(0)" ::: "memory");
  __syncthreads();
  if (tid == 0) __hip_atomic_fetch_add((GAS unsigned*)(ws + WS_CNT + (d >> 6) * 256), 1u, RLX_AGENT);
  ST(1, 6);
}

extern "C" void kernel_launch(void* const* d_in, const int* in_sizes, int n_in, void* d_out, int out_size, void* d_ws,
                              size_t ws_size, hipStream_t stream) {
  const char* x = (const char*)d_in[0];
  char* ws = (char*)d_ws;
  (void)hipFuncSetAttribute((const void*)k1_kernel, hipFuncAttributeMaxDynamicSharedMemorySize, K1_LDS);
  (void)hipFuncSetAttribute((const void*)k2_kernel, hipFuncAttributeMaxDynamicSharedMemorySize, K2_LDS);
  k0_kernel<<<448 + 128 + 56 + 64, 256, K0_LDS, stream>>>(x, (const char*)d_in[1], (const char*)d_in[2], (const char*)d_in[3],
                                                     (const char*)d_in[4], (const char*)d_in[5], (const char*)d_in[6],
                                                     (const char*)d_in[7], (const char*)d_in[12], (const char*)d_in[10], (const char*)d_in[11], ws);
  k1_kernel<<<224, 768, K1_LDS, stream>>>((const char*)d_in[10], (const char*)d_in[11], ws);
  k2_kernel<<<448 + 224, 256, K2_LDS, stream>>>((const char*)d_in[8], x, (char*)d_out, ws);
}
```

```cpp
#include <hip/hip_runtime.h>
#include <hip/hip_bf16.h>
#include <stdint.h>

typedef __attribute__((ext_vector_type(8))) __bf16 bf16x8;
typedef __attribute__((ext_vector_type(2))) __bf16 bf16x2;
typedef __attribute__((ext_vector_type(8))) _Float16 f16x8;
typedef __attribute__((ext_vector_type(4))) _Float16 f16x4;
typedef __attribute__((ext_vector_type(2))) _Float16 f16x2;
typedef __attribute__((ext_vector_type(4))) float f32x4;
typedef __attribute__((ext_vector_type(2))) float f32x2;
typedef __attribute__((ext_vector_type(4))) unsigned u32x4;
typedef __attribute__((ext_vector_type(2))) unsigned u32x2;
typedef __attribute__((ext_vector_type(4))) short s16x4;
#define LAS __attribute__((address_space(3)))
#define GAS __attribute__((address_space(1)))
#define RLX_AGENT __ATOMIC_RELAXED, __HIP_MEMORY_SCOPE_AGENT

#define SEQ 2048
#define DM 448
#define NS 256
#define FF 1792

#define WS_H 0u
#define WS_WD 7340032u
#define WS_XT (WS_WD + 1605632u)
#define WS_YT (WS_XT + 1835008u)
#define WS_XN (WS_YT + 1835008u)
#define WS_KR (WS_XN + 1835008u)
#define WS_KS (WS_KR + 1835008u)
#define WS_CNT (WS_KS + 2048u)

__device__ __forceinline__ unsigned pk_bf16(float a, float b) {
  bf16x2 v = {(__bf16)a, (__bf16)b};
  return __builtin_bit_cast(unsigned, v);
}
__device__ __forceinline__ unsigned pk_bf16o(float a, float b) {
  unsigned r;
  asm("v_cvt_pk_bf16_f32 %0, %1, %2" : "=v"(r) : "v"(a), "v"(b));
  return r;
}
__device__ __forceinline__ unsigned pk_f16(float a, float b) {
  f16x2 v = {(_Float16)a, (_Float16)b};
  return __builtin_bit_cast(unsigned, v);
}
__device__ __forceinline__ float bf_lo(unsigned u) { return __uint_as_float(u << 16); }
__device__ __forceinline__ float bf_hi(unsigned u) { return __uint_as_float(u & 0xffff0000u); }
__device__ __forceinline__ float rbf(float x) { return (float)(__bf16)x; }
__device__ __forceinline__ float bf1(const void* p, int i) {
  return __uint_as_float(((unsigned)((const unsigned short*)p)[i]) << 16);
}
__device__ __forceinline__ u32x2 tr_read(const char* lds) {
  s16x4 v = __builtin_amdgcn_ds_read_tr16_b64_v4i16((LAS s16x4*)lds);
  return __builtin_bit_cast(u32x2, v);
}
#define ST(kid, i)
#define WAIT_V(n) asm volatile("s_waitcnt vmcnt(" #n ")" ::: "memory")
#define BAR() asm volatile("s_waitcnt lgkmcnt(0)\n\ts_barrier" ::: "memory")
__device__ __forceinline__ void glds16(const char* sbase, unsigned voff, unsigned lds_dst) {
  unsigned keep;
  asm volatile("s_mov_b32 %0, m0\n\ts_mov_b32 m0, %3\n\ts_nop 0\n\tglobal_load_lds_dwordx4 %1, %2\n\ts_mov_b32 m0, %0"
               : "=&s"(keep) : "v"(voff), "s"(sbase), "s"(lds_dst) : "memory");
}
__device__ __forceinline__ float sum_q(float v) {
  unsigned u = __float_as_uint(v);
  auto a = __builtin_amdgcn_permlane16_swap(u, u, false, false);
  float s = __uint_as_float(a[0]) + __uint_as_float(a[1]);
  u = __float_as_uint(s);
  auto b = __builtin_amdgcn_permlane32_swap(u, u, false, false);
  return __uint_as_float(b[0]) + __uint_as_float(b[1]);
}

#define KG_TW 4608
#define KG_KC (4 * KG_TW)
#define KG_MISC (KG_KC + 1536)
#define K0_LDS 28672

__device__ __forceinline__ f32x2 cmul2(f32x2 a, f32x2 b, f32x2 bs) {
  f32x2 t = a.xx * b;
  return __builtin_elementwise_fma(a.yy, bs, t);
}
__device__ __forceinline__ f32x2 rot(f32x2 b) { return (f32x2){-b.y, b.x}; }
__device__ __forceinline__ f32x2 csq(f32x2 a) {
  float r = fmaf(a.x, a.x, -(a.y * a.y)), i = (a.x + a.x) * a.y;
  asm volatile("" : "+v"(r), "+v"(i));
  return (f32x2){r, i};
}
__device__ __forceinline__ f32x2 cexp_tau(float re_l2, float rev_hi, float rev_lo, float tau) {
  float mag = __builtin_amdgcn_exp2f(re_l2 * tau);
  float fr = __builtin_amdgcn_fractf(rev_hi * tau) + rev_lo * tau;
  return (f32x2){mag * __builtin_amdgcn_cosf(fr), mag * __builtin_amdgcn_sinf(fr)};
}
#define DPP(v, ctrl) __int_as_float(__builtin_amdgcn_update_dpp(0, __float_as_int(v), ctrl, 0xf, 0xf, false))

__global__ __launch_bounds__(256) void k0_kernel(const char* __restrict__ x, const char* __restrict__ Are, const char* __restrict__ Aim,
                                                 const char* __restrict__ Bre, const char* __restrict__ Bim,
                                                 const char* __restrict__ Cre, const char* __restrict__ Cim,
                                                 const char* __restrict__ logdt, const char* __restrict__ wd,
                                                 const char* __restrict__ wg, const char* __restrict__ wu, char* __restrict__ ws) {
  extern __shared__ __attribute__((aligned(16))) char lds[];
  const int tid = threadIdx.x, lane = tid & 63;
  const int wid = __builtin_amdgcn_readfirstlane(tid >> 6);
  const int r = lane & 15, q = lane >> 4, qq = (lane & 15) >> 2, p = lane & 3;
  const int bid = blockIdx.x;
  if (bid >= 632) {
    const int xcd = bid & 7, part = (bid - 632) >> 3;
    const char* base = (tid < 128 ? wg : wu) + (size_t)(14 * part) * (FF * 2) + (xcd >> 2) * 1792 + (tid & 127) * 16;
    u32x4 acc = {0, 0, 0, 0};
    if ((tid & 127) < 112) {
      u32x4 v[14];
#pragma unroll
      for (int i = 0; i < 14; ++i) v[i] = *(const u32x4*)(base + (size_t)i * (FF * 2));
#pragma unroll
      for (int i = 0; i < 14; ++i) acc ^= v[i];
    }
    asm volatile("" :: "v"(acc));
    return;
  }
  if (bid >= 576) {
    if (bid == 576 && tid < 7) *(unsigned*)(ws + WS_CNT + tid * 256) = 0u;
    const unsigned lbase = (unsigned)(size_t)(LAS char*)lds;
    const int u = (bid - 576) * 4 + wid, s2 = u >> 2, cq = u & 3;
    const char* src = wd + (size_t)(32 * s2) * (DM * 2) + cq * 224;
#pragma unroll
    for (int i = 0; i < 7; ++i) glds16(src + i * 32, (lane >> 1) * (DM * 2) + (lane & 1) * 16, lbase + wid * 7168 + i * 1024);
    WAIT_V(0);
    const char* bp = lds + wid * 7168 + (4 * q + qq) * 32 + p * 8;
#pragma unroll
    for (int i = 0; i < 7; ++i) {
      u32x2 lo = tr_read(bp + i * 1024), hi = tr_read(bp + i * 1024 + 512);
      *(u32x4*)(ws + WS_WD + (size_t)(s2 * 28 + 7 * cq + i) * 1024 + lane * 16) = (u32x4){lo[0], lo[1], hi[0], hi[1]};
    }
    return;
  }
  if (bid >= 448) {
    const int tok0 = (bid - 448) * 16;
    const size_t rowo = (size_t)(tok0 + r) * (DM * 2) + q * 16 + wid * 64;
    u32x4 xr[4];
#pragma unroll
    for (int i = 0; i < 3; ++i) xr[i] = *(const u32x4*)(x + rowo + i * 256);
    xr[3] = (u32x4){0, 0, 0, 0};
    if (wid < 2) xr[3] = *(const u32x4*)(x + rowo + 768);
    float s1 = 0, s2 = 0;
#pragma unroll
    for (int i = 0; i < 4; ++i)
#pragma unroll
      for (int w = 0; w < 4; ++w) {
        const unsigned v = xr[i][w];
        const float lo = bf_lo(v), hi = bf_hi(v);
        s1 += lo + hi;
        s2 = fmaf(lo, lo, fmaf(hi, hi, s2));
      }
    s1 = sum_q(s1); s2 = sum_q(s2);
    if (q == 0) *(f32x2*)(lds + (wid * 16 + r) * 8) = (f32x2){s1, s2};
    __syncthreads();
    float t1 = 0, t2 = 0;
#pragma unroll
    for (int w = 0; w < 4; ++w) { const f32x2 v = *(const f32x2*)(lds + (w * 16 + r) * 8); t1 += v.x; t2 += v.y; }
    const float mean = t1 * (1.0f / DM);
    const float var = fmaxf(t2 * (1.0f / DM) - mean * mean, 0.0f);
    const float mb = rbf(mean);
    const float rs = 1.0f / rbf(sqrtf(rbf(rbf(var) + 1e-5f)));
    char* XT = lds + 1024 + wid * 4096;
#pragma unroll
    for (int i = 0; i < 4; ++i) {
      if (i < 3 || wid < 2) {
        u32x4 o;
#pragma unroll
        for (int w = 0; w < 4; ++w) {
          const unsigned v = xr[i][w];
          const unsigned d = pk_bf16o(bf_lo(v) - mb, bf_hi(v) - mb);
          o[w] = pk_bf16o(bf_lo(d) * rs, bf_hi(d) * rs);
        }
        *(u32x4*)(ws + WS_XN + rowo + i * 256) = o;
        *(u32x4*)(XT + i * 1024 + lane * 16) = o;
      }
    }
    __builtin_amdgcn_wave_barrier();
#pragma unroll
    for (int i = 0; i < 4; ++i) {
      if (i < 3 || wid < 2) {
#pragma unroll
        for (int cb = 0; cb < 2; ++cb) {
          u32x2 v = tr_read(XT + i * 1024 + ((2 * cb + (p >> 1)) * 16 + 4 * q + qq) * 16 + 8 * (p & 1));
          u32x2 o = {pk_f16(bf_lo(v[0]), bf_hi(v[0])), pk_f16(bf_lo(v[1]), bf_hi(v[1]))};
          *(u32x2*)(ws + WS_XT + ((size_t)(32 * (wid + 4 * i) + 16 * cb + r) * SEQ + tok0 + 4 * q) * 2) = o;
        }
      }
    }
    return;
  }
  const int d = bid;
  ST(2, 0);
  const int n = d * NS + tid;
  const float a_re = bf1(Are, n), a_im = bf1(Aim, n), b_re = bf1(Bre, n), b_im = bf1(Bim, n), cr = bf1(Cre, n), ci = bf1(Cim, n);
  const float ldt = bf1(logdt, d);
  u32x4 kidx;
#pragma unroll
  for (int w = 0; w < 4; ++w) {
    unsigned e[2];
#pragma unroll
    for (int h = 0; h < 2; ++h) {
      const int lag = 2047 - 8 * tid - 2 * w - h;
      const int lb = (int)rbf((float)lag);
      const int seg = (31 - __builtin_clz(lb | 128)) - 7;
      const int o = lb & ~(seg ? (128 << seg) : 0);
      const int row = (seg ? 8 + 8 * seg : 0) + ((o & 7) >> seg) + ((o >> 7) << 3 >> seg);
      e[h] = (row * 16 + ((lb >> 3) & 15)) * 2;
    }
    kidx[w] = e[0] | (e[1] << 16);
  }
  asm volatile("" : "+v"(kidx));
  {
    const float dt = rbf(__expf(ldt));
    const float re = dt * a_re, im = dt * a_im, br = dt * b_re, bi = dt * b_im;
    const float CBr = cr * br - ci * bi, CBi = cr * bi + ci * br;
    const float re_l2 = re * 1.44269504088896340736f;
    const float rev_hi = im * 0.15915494309189535f;
    const float rev_lo = fmaf(im, 0.15915494309189535f, -rev_hi) + im * 6.4206e-9f;
    const float sig = fmaxf(1.0f, __builtin_amdgcn_exp2f(re_l2 * 120.0f));
    const float gmx = fmaxf(1.0f, __builtin_amdgcn_exp2f(re_l2 * 2048.0f));
    const float cb1 = (fabsf(CBr) + fabsf(CBi)) * gmx;
    float mx = cb1 * sig, sm = cb1;
    mx = fmaxf(mx, DPP(mx, 0x111)); sm += DPP(sm, 0x111);
    mx = fmaxf(mx, DPP(mx, 0x112)); sm += DPP(sm, 0x112);
    mx = fmaxf(mx, DPP(mx, 0x114)); sm += DPP(sm, 0x114);
    mx = fmaxf(mx, DPP(mx, 0x118)); sm += DPP(sm, 0x118);
    mx = fmaxf(mx, DPP(mx, 0x142)); sm += DPP(sm, 0x142);
    mx = fmaxf(mx, DPP(mx, 0x143)); sm += DPP(sm, 0x143);
    if (lane == 63) *(float*)(lds + KG_MISC + wid * 4) = sm;
    mx = __int_as_float(__builtin_amdgcn_readlane(__float_as_int(mx), 63));
    const int ew = __builtin_amdgcn_frexp_expf(mx);
    const float scw = __builtin_amdgcn_ldexpf(sig, 15 - ew);
    const float unsc = __builtin_amdgcn_ldexpf(1.0f, ew - 30);
    const f32x2 z1 = cexp_tau(re_l2, rev_hi, rev_lo, 1.0f);
    const f32x2 z8 = cexp_tau(re_l2, rev_hi, rev_lo, 8.0f);
    const f32x2 z128 = cexp_tau(re_l2, rev_hi, rev_lo, 128.0f);
    ST(2, 1);
    char* T = lds + wid * KG_TW;
    char* Tw = T + lane * 4;
    const char* Tr = T + r * 288 + q * 16;
    auto put = [&](int row, f32x2 g) { *(unsigned*)(Tw + row * 288) = pk_f16(g.x, g.y); };
    {
      const f32x2 z8s = rot(z8);
      f32x2 e = {32768.0f * __builtin_amdgcn_rcpf(sig), 0.0f};
#pragma unroll
      for (int j = 0; j < 16; ++j) {
        put(j, e);
        if (j < 15) e = cmul2(e, z8, z8s);
      }
    }
    __builtin_amdgcn_wave_barrier();
    f16x8 E[4];
#pragma unroll
    for (int s = 0; s < 4; ++s) E[s] = *(const f16x8*)(Tr + s * 64);
    __builtin_amdgcn_wave_barrier();
    const f32x2 y1 = {z1.x, -z1.y}, y128 = {z128.x, -z128.y};
    const f32x2 y1s = rot(y1), y128s = rot(y128);
    const f32x2 H0 = {CBr * scw, -CBi * scw};
    const f32x2 y2 = csq(y1), y4 = csq(y2), y256 = csq(y128), y512 = csq(y256), y1024 = csq(y512);
    f32x4 kacc[3];
    auto mm = [&](int t) {
      __builtin_amdgcn_wave_barrier();
      f32x4 a = {0, 0, 0, 0};
#pragma unroll
      for (int s = 0; s < 4; ++s) {
        f16x8 g = *(const f16x8*)(Tr + s * 64);
        a = __builtin_amdgcn_mfma_f32_16x16x32_f16(E[s], g, a, 0, 0, 0);
      }
      kacc[t] = a;
      __builtin_amdgcn_wave_barrier();
    };
    {
      f32x2 g = H0;
#pragma unroll
      for (int i = 0; i < 8; ++i) {
        put(i, g); put(8 + i, cmul2(g, y128, y128s));
        if (i < 7) g = cmul2(g, y1, y1s);
      }
    }
    mm(0);
    {
      const f32x2 y2s = rot(y2);
      f32x2 g = cmul2(H0, y256, rot(y256));
#pragma unroll
      for (int i = 0; i < 4; ++i) {
        put(i, g); put(4 + i, cmul2(g, y128, y128s));
        if (i < 3) g = cmul2(g, y2, y2s);
      }
      f32x2 ga = cmul2(H0, y512, rot(y512)), gb = cmul2(ga, y4, rot(y4));
#pragma unroll
      for (int i = 0; i < 4; ++i) {
        put(8 + 2 * i, ga); put(9 + 2 * i, gb);
        if (i < 3) { ga = cmul2(ga, y128, y128s); gb = cmul2(gb, y128, y128s); }
      }
    }
    mm(1);
    {
      f32x2 g = cmul2(H0, y1024, rot(y1024));
#pragma unroll
      for (int i = 0; i < 9; ++i) {
        put(i, g);
        if (i < 8) g = cmul2(g, y128, y128s);
      }
    }
    mm(2);
#pragma unroll
    for (int t = 0; t < 3; ++t) *(f32x4*)(T + ((16 * t + r) * 16 + 4 * q) * 4) = kacc[t] * unsc;
  }
  ST(2, 2);
  __syncthreads();
  {
    const f32x4 ms = *(const f32x4*)(lds + KG_MISC);
    const int ek = __builtin_amdgcn_frexp_expf((ms[0] + ms[1]) + (ms[2] + ms[3]));
    if (tid == 255) *(float*)(ws + WS_KS + d * 4) = __builtin_amdgcn_ldexpf(1.0f, ek - 15);
    if (tid < 192) {
      const float ksc = __builtin_amdgcn_ldexpf(1.0f, 15 - ek);
      const char* kp = lds + tid * 16;
      f32x4 v = ((*(const f32x4*)kp + *(const f32x4*)(kp + KG_TW)) + (*(const f32x4*)(kp + 2 * KG_TW) + *(const f32x4*)(kp + 3 * KG_TW))) * ksc;
      *(u32x2*)(lds + KG_KC + tid * 8) = (u32x2){pk_f16(v[0], v[1]), pk_f16(v[2], v[3])};
    }
  }
  __syncthreads();
  {
    const char* kc = lds + KG_KC;
    u32x4 o;
#pragma unroll
    for (int w = 0; w < 4; ++w) {
      const unsigned iv = kidx[w];
      f16x2 h;
      h.x = *(const _Float16*)(kc + (iv & 0xffffu));
      h.y = *(const _Float16*)(kc + (iv >> 16));
      o[w] = __builtin_bit_cast(unsigned, h);
    }
    *(u32x4*)(ws + WS_KR + (size_t)d * 4096 + tid * 16) = o;
  }
  ST(2, 3);
}

#define K1_SLOT 49152
#define K1_LDS (3 * K1_SLOT)

__global__ __launch_bounds__(768, 3) void k1_kernel(const char* __restrict__ wg, const char* __restrict__ wu, char* __restrict__ ws) {
  extern __shared__ __attribute__((aligned(16))) char lds[];
  const int tid = threadIdx.x, lane = tid & 63;
  const int wid = __builtin_amdgcn_readfirstlane(tid >> 6);
  const int bid = blockIdx.x;
  const unsigned lbase = (unsigned)(size_t)(LAS char*)lds;
  const int r = lane & 15, q = lane >> 4, qq = (lane & 15) >> 2, p = lane & 3;
  const int xcd = bid & 7, idx = bid >> 3;
  const int mt = 4 * (xcd & 3) + (idx & 3);
  const int jt = 7 * (xcd >> 2) + (idx >> 2);
  const int m = (wid >> 2) & 1, n = wid & 3;
  ST(0, 0);
  if (wid >= 8) {
    const int pw = wid - 8;
    const unsigned aoff = (lane >> 3) * (DM * 2) + (((lane & 7) ^ (lane >> 3)) * 16);
    const char* xsrc = ws + WS_XN + (size_t)(mt * 128 + pw * 16) * (DM * 2);
    const unsigned boff = (8 * pw + q) * (FF * 2) + ((((r >> 1) ^ ((4 * (pw & 1) + q) & 7)) * 2 + (r & 1)) * 16);
    const char* gsrc = wg + jt * 256;
    const char* usrc = wu + jt * 256;
    auto dma_chunk = [&](int c, int slot) {
      const unsigned dst = lbase + slot * K1_SLOT + pw * 1024;
      const size_t wo = (size_t)c * (64 * FF * 2);
#pragma unroll
      for (int h = 0; h < 2; ++h) {
        glds16(xsrc + c * 128 + h * 64 * DM * 2, aoff, dst + pw * 1024 + h * 8192);
        glds16(xsrc + c * 128 + h * 64 * DM * 2 + 8 * DM * 2, aoff, dst + pw * 1024 + h * 8192 + 1024);
        glds16(gsrc + wo + h * 4 * FF * 2, boff, dst + 16384 + h * 4096);
        glds16(usrc + wo + h * 4 * FF * 2, boff, dst + 32768 + h * 4096);
        glds16(gsrc + wo + (32 + h * 4) * FF * 2, boff, dst + 16384 + 8192 + h * 4096);
        glds16(usrc + wo + (32 + h * 4) * FF * 2, boff, dst + 32768 + 8192 + h * 4096);
      }
    };
    dma_chunk(0, 0); dma_chunk(1, 1);
    WAIT_V(12);
    BAR();
    dma_chunk(2, 2);
    int slot = 0;
#pragma unroll 1
    for (int c = 0; c < 6; ++c) {
      if (c < 5) WAIT_V(12); else WAIT_V(0);
      BAR();
      if (c < 4) dma_chunk(c + 3, slot);
      slot = slot == 2 ? 0 : slot + 1;
    }
    return;
  }
  f32x4 acc[4][4];
#pragma unroll
  for (int i = 0; i < 4; ++i)
#pragma unroll
    for (int j = 0; j < 4; ++j) acc[i][j] = (f32x4){0, 0, 0, 0};
  const int x7 = (4 * q + qq) & 7;
  const int brd0 = 16384 + (4 * q + qq) * 256 + (((2 * n) ^ x7) * 32) + p * 8;
  const int brd1 = brd0 ^ 32;
  const int ard0 = (4 * m) * 2048 + (r >> 3) * 1024 + (r & 7) * 128 + ((q ^ (r & 7)) * 16);
  const int ard1 = ard0 ^ 64;
  u32x4 a[2][4], b[2][4];
  auto rdA = [&](int ks, const char* S, int i) { a[ks][i] = *(const u32x4*)(S + (ks ? ard1 : ard0) + i * 2048); };
  auto rdB = [&](int ks, const char* S, int t) {
    const char* bp = S + ((t & 1) ? brd1 : brd0) + (t >> 1) * 16384 + ks * 8192;
    u32x2 lo = tr_read(bp), hi = tr_read(bp + 4096);
    b[ks][t] = (u32x4){lo[0], lo[1], hi[0], hi[1]};
  };
  auto mm = [&](int ks, const char* S) {
#pragma unroll
    for (int t = 0; t < 4; ++t) {
#pragma unroll
      for (int i = 0; i < 4; ++i) {
        acc[t][i] = __builtin_amdgcn_mfma_f32_16x16x32_bf16(__builtin_bit_cast(bf16x8, b[ks][t]), __builtin_bit_cast(bf16x8, a[ks][i]), acc[t][i], 0, 0, 0);
        const int st = 4 * t + i;
        if (st < 8) { if (st & 1) rdB(1 - ks, S, st >> 1); else rdA(1 - ks, S, st >> 1); }
        __builtin_amdgcn_sched_barrier(0);
      }
    }
    __builtin_amdgcn_s_waitcnt(0xC07F);
  };
  BAR();
  ST(0, 2);
#pragma unroll
  for (int t = 0; t < 4; ++t) { rdA(0, lds, t); rdB(0, lds, t); }
  __builtin_amdgcn_s_waitcnt(0xC07F);
  int slot = 0;
#pragma unroll 1
  for (int c = 0; c < 7; ++c) {
    mm(0, lds + slot * K1_SLOT);
    if (c < 6) {
      BAR();
      ST(0, 3 + c);
      slot = slot == 2 ? 0 : slot + 1;
    }
    mm(1, lds + slot * K1_SLOT);
  }
  ST(0, 9);
#pragma unroll
  for (int i = 0; i < 4; ++i) {
    u32x4 hv;
#pragma unroll
    for (int t = 0; t < 2; ++t) {
      const f32x4 g = acc[t][i], u = acc[t + 2][i];
      unsigned g01 = pk_bf16o(g[0], g[1]), g23 = pk_bf16o(g[2], g[3]);
      unsigned u01 = pk_bf16o(u[0], u[1]), u23 = pk_bf16o(u[2], u[3]);
      hv[2 * t] = pk_bf16(bf_lo(g01) * bf_lo(u01), bf_hi(g01) * bf_hi(u01));
      hv[2 * t + 1] = pk_bf16(bf_lo(g23) * bf_lo(u23), bf_hi(g23) * bf_hi(u23));
    }
    *(u32x4*)(ws + WS_H + ((size_t)(mt * 8 + 4 * m + i) * 56 + 4 * jt + n) * 1024 + lane * 16) = hv;
  }
  ST(0, 10);
}

#define K2_LDS 49152
#define CV_XS 4160
#define CV_KR 32768

__global__ __launch_bounds__(256, 3) void k2_kernel(const char* __restrict__ Dskip, const char* __restrict__ x, char* __restrict__ out, char* ws) {
  extern __shared__ __attribute__((aligned(16))) char lds[];
  const int tid = threadIdx.x, lane = tid & 63;
  const int wid = __builtin_amdgcn_readfirstlane(tid >> 6);
  const int r = lane & 15, q = lane >> 4;
  const int bid = blockIdx.x;
  if (bid >= 448) {
    ST(1, 0);
    const int xcd = bid & 7, idx = (bid - 448) >> 3;
    const int mt = 8 * (xcd & 3) + 4 * (xcd >> 2) + (idx & 3), nt = idx >> 2;
    const int rot0 = 3 * wid + (idx & 3);
    const __amdgpu_buffer_rsrc_t rs = __builtin_amdgcn_make_buffer_rsrc(ws, 0, WS_XT, 0x00020000);
    const int hA = WS_H + ((4 * mt) * 56 + 14 * wid) * 1024;
    const int wB = WS_WD + ((14 * wid) * 28 + 4 * nt) * 1024;
    const unsigned lo16 = lane * 16;
    f32x4 acc[4][4];
    u32x4 a[3][4], b[3][4];
    auto load = [&](int buf, int s) {
      s += rot0; s = s >= 14 ? s - 14 : s;
#pragma unroll
      for (int i = 0; i < 4; ++i) {
        a[buf][i] = __builtin_amdgcn_raw_buffer_load_b128(rs, lo16, hA + (i * 56 + s) * 1024, 0);
        b[buf][i] = __builtin_amdgcn_raw_buffer_load_b128(rs, lo16, wB + (s * 28 + ((i + wid) & 3)) * 1024, 0);
      }
    };
    auto comp = [&](int buf) {
#pragma unroll
      for (int j = 0; j < 4; ++j)
#pragma unroll
        for (int i = 0; i < 4; ++i)
          acc[j][i] = __builtin_amdgcn_mfma_f32_16x16x32_bf16(__builtin_bit_cast(bf16x8, b[buf][j]),
                                                              __builtin_bit_cast(bf16x8, a[buf][i]), acc[j][i], 0, 0, 0);
    };
    __builtin_amdgcn_s_sleep(8);
    load(0, 0); load(1, 1); load(2, 2);
#pragma unroll
    for (int i = 0; i < 4; ++i)
#pragma unroll
      for (int j = 0; j < 4; ++j) acc[i][j] = (f32x4){0, 0, 0, 0};
#pragma unroll 1
    for (int it = 0; it < 9; it += 3) {
      comp(0); load(0, it + 3);
      comp(1); load(1, it + 4);
      comp(2); load(2, it + 5);
    }
    comp(0); load(0, 12);
    comp(1); load(1, 13);
    comp(2); comp(0); comp(1);
    ST(1, 1);
#pragma unroll
    for (int j = 1; j < 4; ++j) {
      char* dp = lds + ((((j + wid) & 3) * 3 + (j - 1)) * 4) * 1024 + lo16;
#pragma unroll
      for (int i = 0; i < 4; ++i) *(f32x4*)(dp + i * 1024) = acc[j][i];
    }
    __builtin_amdgcn_sched_barrier(0);
    u32x2 xv[4];
#pragma unroll
    for (int i = 0; i < 4; ++i) xv[i] = *(const u32x2*)(x + ((size_t)(64 * mt + 16 * i + r) * DM + 64 * nt + 16 * wid + 4 * q) * 2);
    if (wid == 0) {
      GAS unsigned* cnt = (GAS unsigned*)(ws + WS_CNT + nt * 256);
      for (unsigned spins = 0; spins < (1u << 22); ++spins) {
        if ((unsigned)__builtin_amdgcn_readfirstlane(__hip_atomic_load(cnt, RLX_AGENT)) == 64u) break;
        __builtin_amdgcn_s_sleep(16);
      }
    }
    __syncthreads();
    const __amdgpu_buffer_rsrc_t ry = __builtin_amdgcn_make_buffer_rsrc(ws + WS_YT, 0, 1835008, 0x00020000);
    u32x4 yv4[2];
#pragma unroll
    for (int i = 0; i < 2; ++i) {
      const int c = tid + 256 * i;
      yv4[i] = __builtin_amdgcn_raw_buffer_load_b128(ry, ((64 * nt + (c >> 3)) * SEQ + 64 * mt) * 2 + (c & 7) * 16, 0, 16);
    }
#pragma unroll
    for (int sl = 0; sl < 3; ++sl)
#pragma unroll
      for (int i = 0; i < 4; ++i) acc[0][i] += *(const f32x4*)(lds + ((wid * 3 + sl) * 4 + i) * 1024 + lo16);
    __syncthreads();
#pragma unroll
    for (int i = 0; i < 2; ++i) {
      const int c = tid + 256 * i;
      *(u32x4*)(lds + (c >> 3) * 144 + (c & 7) * 16) = yv4[i];
    }
    __syncthreads();
    {
      const int qq = (lane & 15) >> 2, p = lane & 3;
#pragma unroll
      for (int i = 0; i < 4; ++i) {
        const u32x2 yv = tr_read(lds + (16 * wid + 4 * q + qq) * 144 + (16 * i + 4 * p) * 2);
        const unsigned b01 = pk_bf16o(acc[0][i][0], acc[0][i][1]), b23 = pk_bf16o(acc[0][i][2], acc[0][i][3]);
        const unsigned z01 = pk_bf16o(bf_lo(xv[i][0]) + bf_lo(yv[0]), bf_hi(xv[i][0]) + bf_hi(yv[0]));
        const unsigned z23 = pk_bf16o(bf_lo(xv[i][1]) + bf_lo(yv[1]), bf_hi(xv[i][1]) + bf_hi(yv[1]));
        u32x2 o = {pk_bf16(bf_lo(z01) + bf_lo(b01), bf_hi(z01) + bf_hi(b01)), pk_bf16(bf_lo(z23) + bf_lo(b23), bf_hi(z23) + bf_hi(b23))};
        *(u32x2*)(out + ((size_t)(64 * mt + 16 * i + r) * DM + 64 * nt + 16 * wid + 4 * q) * 2) = o;
      }
    }
    ST(1, 2);
    return;
  }
  const int d = bid;
  __builtin_amdgcn_s_setprio(2);
  ST(1, 0);
  const char* xt = ws + WS_XT + (size_t)d * SEQ * 2;
  const u32x4 xa = *(const u32x4*)(xt + tid * 16);
  const u32x2 xb = *(const u32x2*)(xt + (tid < 255 ? tid * 16 + 16 : 0));
  const u32x4 kr = *(const u32x4*)(ws + WS_KR + (size_t)d * 4096 + tid * 16);
  const float kun = *(const float*)(ws + WS_KS + d * 4);
  const float dsk = bf1(Dskip, d);
  if (tid >= 224) *(u32x4*)(lds + CV_KR + 4096 + (tid - 224) * 16) = (u32x4){0, 0, 0, 0};
  {
    char* xc = lds + tid * 16 + 32;
    const unsigned s0 = __builtin_amdgcn_alignbyte(xa[1], xa[0], 2), s1 = __builtin_amdgcn_alignbyte(xa[2], xa[1], 2),
                   s2 = __builtin_amdgcn_alignbyte(xa[3], xa[2], 2), s3 = __builtin_amdgcn_alignbyte(xb[0], xa[3], 2),
                   s4 = __builtin_amdgcn_alignbyte(xb[1], xb[0], 2);
    *(u32x4*)(xc) = xa;
    *(u32x4*)(xc + CV_XS) = (u32x4){s0, s1, s2, s3};
    *(u32x4*)(xc + 2 * CV_XS) = (u32x4){xa[1], xa[2], xa[3], xb[0]};
    *(u32x4*)(xc + 3 * CV_XS) = (u32x4){s1, s2, s3, s4};
    if (tid == 0) {
      u32x4 z = {0, 0, 0, 0};
      *(u32x4*)(lds) = z; *(u32x4*)(lds + 16) = z;
      *(u32x4*)(lds + CV_XS) = z; *(u32x4*)(lds + CV_XS + 16) = (u32x4){0, 0, 0, xa[0] << 16};
      *(u32x4*)(lds + 2 * CV_XS) = z; *(u32x4*)(lds + 2 * CV_XS + 16) = (u32x4){0, 0, 0, xa[0]};
      *(u32x4*)(lds + 3 * CV_XS) = z; *(u32x4*)(lds + 3 * CV_XS + 16) = (u32x4){0, 0, xa[0] << 16, s0};
    }
    *(u32x4*)(lds + CV_KR + tid * 16) = kr;
  }
  __syncthreads();
  ST(1, 4);
  f32x4 acc[8];
  {
    const int e0 = 8 * q + r + 1;
    const char* xp = lds + (e0 & 3) * CV_XS + (e0 & ~3) * 2 + wid * 128;
    const char* kp = lds + CV_KR + (2032 + 64 * wid - 16 * r + 8 * q) * 2;
#pragma unroll
    for (int rr = 0; rr < 2; ++rr) {
      f16x8 G[8];
#pragma unroll
      for (int dl = 0; dl < 8; ++dl) G[dl] = *(const f16x8*)(kp + rr * 64 - dl * 512);
#pragma unroll
      for (int i = 0; i < 8; ++i) {
        const char* fp = xp + (rr + 8 * i) * 64;
        u32x2 f0 = *(const u32x2*)fp, f1 = *(const u32x2*)(fp + 8);
        f16x8 Fr = __builtin_bit_cast(f16x8, (u32x4){f0[0], f0[1], f1[0], f1[1]});
#pragma unroll
        for (int nn = i; nn < 8; ++nn)
          acc[nn] = __builtin_amdgcn_mfma_f32_16x16x32_f16(Fr, G[nn - i], (rr == 0 && i == 0) ? (f32x4){0, 0, 0, 0} : acc[nn], 0, 0, 0);
      }
    }
  }
  ST(1, 5);
  u32x2 xo[2];
#pragma unroll
  for (int i = 0; i < 2; ++i) xo[i] = *(const u32x2*)(lds + (256 * (2 * wid + i) + 16 * r + 4 * q + 16) * 2);
  __syncthreads();
#pragma unroll
  for (int j = 0; j < 8; ++j) *(f32x4*)(lds + (j * 4 + wid) * 1024 + lane * 16) = acc[j];
  __syncthreads();
  u32x2 yv[2];
#pragma unroll
  for (int i = 0; i < 2; ++i) {
    const char* rp = lds + (2 * wid + i) * 4096 + lane * 16;
    f32x4 o = ((*(const f32x4*)rp + *(const f32x4*)(rp + 1024)) + (*(const f32x4*)(rp + 2048) + *(const f32x4*)(rp + 3072))) * kun;
    const u32x2 xoi = xo[i];
    f16x4 xv = __builtin_bit_cast(f16x4, xoi);
    const unsigned c01 = pk_bf16o(o[0], o[1]), c23 = pk_bf16o(o[2], o[3]);
    const unsigned s01 = pk_bf16o((float)xv[0] * dsk, (float)xv[1] * dsk), s23 = pk_bf16o((float)xv[2] * dsk, (float)xv[3] * dsk);
    yv[i] = (u32x2){pk_bf16(bf_lo(c01) + bf_lo(s01), bf_hi(c01) + bf_hi(s01)), pk_bf16(bf_lo(c23) + bf_lo(s23), bf_hi(c23) + bf_hi(s23))};
  }
  {
    auto e0 = __builtin_amdgcn_permlane16_swap(yv[0][0], yv[1][0], false, false);
    auto e1 = __builtin_amdgcn_permlane16_swap(yv[0][1], yv[1][1], false, false);
    const __amdgpu_buffer_rsrc_t ry = __builtin_amdgcn_make_buffer_rsrc(ws + WS_YT, 0, 1835008, 0x00020000);
    __builtin_amdgcn_raw_buffer_store_b128((u32x4){e0[0], e1[0], e0[1], e1[1]}, ry,
                                           (d * SEQ + 256 * (2 * wid + (q & 1)) + 16 * r + 8 * (q >> 1)) * 2, 0, 16);
  }
  asm volatile("s_waitcnt vmcnt(0)" ::: "memory");
  __syncthreads();
  if (tid == 0) __hip_atomic_fetch_add((GAS unsigned*)(ws + WS_CNT + (d >> 6) * 256), 1u, RLX_AGENT);
  ST(1, 6);
}

extern "C" void kernel_launch(void* const* d_in, const int* in_sizes, int n_in, void* d_out, int out_size, void* d_ws,
                              size_t ws_size, hipStream_t stream) {
  const char* x = (const char*)d_in[0];
  char* ws = (char*)d_ws;
  (void)hipFuncSetAttribute((const void*)k1_kernel, hipFuncAttributeMaxDynamicSharedMemorySize, K1_LDS);
  (void)hipFuncSetAttribute((const void*)k2_kernel, hipFuncAttributeMaxDynamicSharedMemorySize, K2_LDS);
  k0_kernel<<<448 + 128 + 56 + 256, 256, K0_LDS, stream>>>(x, (const char*)d_in[1], (const char*)d_in[2], (const char*)d_in[3],
                                                     (const char*)d_in[4], (const char*)d_in[5], (const char*)d_in[6],
                                                     (const char*)d_in[7], (const char*)d_in[12], (const char*)d_in[10], (const char*)d_in[11], ws);
  k1_kernel<<<224, 768, K1_LDS, stream>>>((const char*)d_in[10], (const char*)d_in[11], ws);
  k2_kernel<<<448 + 224, 256, K2_LDS, stream>>>((const char*)d_in[8], x, (char*)d_out, ws);
}
```

```cpp
#include <hip/hip_runtime.h>
#include <hip/hip_bf16.h>
#include <stdint.h>

typedef __attribute__((ext_vector_type(8))) __bf16 bf16x8;
typedef __attribute__((ext_vector_type(2))) __bf16 bf16x2;
typedef __attribute__((ext_vector_type(8))) _Float16 f16x8;
typedef __attribute__((ext_vector_type(4))) _Float16 f16x4;
typedef __attribute__((ext_vector_type(2))) _Float16 f16x2;
typedef __attribute__((ext_vector_type(4))) float f32x4;
typedef __attribute__((ext_vector_type(2))) float f32x2;
typedef __attribute__((ext_vector_type(4))) unsigned u32x4;
typedef __attribute__((ext_vector_type(2))) unsigned u32x2;
typedef __attribute__((ext_vector_type(4))) short s16x4;
#define LAS __attribute__((address_space(3)))
#define GAS __attribute__((address_space(1)))
#define RLX_AGENT __ATOMIC_RELAXED, __HIP_MEMORY_SCOPE_AGENT

#define SEQ 2048
#define DM 448
#define NS 256
#define FF 1792

#define WS_H 0u
#define WS_WD 7340032u
#define WS_XT (WS_WD + 1605632u)
#define WS_YT (WS_XT + 1835008u)
#define WS_XN (WS_YT + 1835008u)
#define WS_KR (WS_XN + 1835008u)
#define WS_KS (WS_KR + 1835008u)
#define WS_CNT (WS_KS + 2048u)

__device__ __forceinline__ unsigned pk_bf16(float a, float b) {
  bf16x2 v = {(__bf16)a, (__bf16)b};
  return __builtin_bit_cast(unsigned, v);
}
__device__ __forceinline__ unsigned pk_bf16o(float a, float b) {
  unsigned r;
  asm("v_cvt_pk_bf16_f32 %0, %1, %2" : "=v"(r) : "v"(a), "v"(b));
  return r;
}
__device__ __forceinline__ unsigned pk_f16(float a, float b) {
  f16x2 v = {(_Float16)a, (_Float16)b};
  return __builtin_bit_cast(unsigned, v);
}
__device__ __forceinline__ float bf_lo(unsigned u) { return __uint_as_float(u << 16); }
__device__ __forceinline__ float bf_hi(unsigned u) { return __uint_as_float(u & 0xffff0000u); }
__device__ __forceinline__ float rbf(float x) { return (float)(__bf16)x; }
__device__ __forceinline__ float bf1(const void* p, int i) {
  return __uint_as_float(((unsigned)((const unsigned short*)p)[i]) << 16);
}
__device__ __forceinline__ u32x2 tr_read(const char* lds) {
  s16x4 v = __builtin_amdgcn_ds_read_tr16_b64_v4i16((LAS s16x4*)lds);
  return __builtin_bit_cast(u32x2, v);
}
#define ST(kid, i)
#define WAIT_V(n) asm volatile("s_waitcnt vmcnt(" #n ")" ::: "memory")
#define BAR() asm volatile("s_waitcnt lgkmcnt(0)\n\ts_barrier" ::: "memory")
__device__ __forceinline__ void glds16(const char* sbase, unsigned voff, unsigned lds_dst) {
  unsigned keep;
  asm volatile("s_mov_b32 %0, m0\n\ts_mov_b32 m0, %3\n\ts_nop 0\n\tglobal_load_lds_dwordx4 %1, %2\n\ts_mov_b32 m0, %0"
               : "=&s"(keep) : "v"(voff), "s"(sbase), "s"(lds_dst) : "memory");
}
__device__ __forceinline__ float sum_q(float v) {
  unsigned u = __float_as_uint(v);
  auto a = __builtin_amdgcn_permlane16_swap(u, u, false, false);
  float s = __uint_as_float(a[0]) + __uint_as_float(a[1]);
  u = __float_as_uint(s);
  auto b = __builtin_amdgcn_permlane32_swap(u, u, false, false);
  return __uint_as_float(b[0]) + __uint_as_float(b[1]);
}

#define KG_TW 4608
#define KG_KC (4 * KG_TW)
#define KG_MISC (KG_KC + 1536)
#define K0_LDS 28672

__device__ __forceinline__ f32x2 cmul2(f32x2 a, f32x2 b, f32x2 bs) {
  f32x2 t = a.xx * b;
  return __builtin_elementwise_fma(a.yy, bs, t);
}
__device__ __forceinline__ f32x2 rot(f32x2 b) { return (f32x2){-b.y, b.x}; }
__device__ __forceinline__ f32x2 csq(f32x2 a) {
  float r = fmaf(a.x, a.x, -(a.y * a.y)), i = (a.x + a.x) * a.y;
  asm volatile("" : "+v"(r), "+v"(i));
  return (f32x2){r, i};
}
__device__ __forceinline__ f32x2 cexp_tau(float re_l2, float rev_hi, float rev_lo, float tau) {
  float mag = __builtin_amdgcn_exp2f(re_l2 * tau);
  float fr = __builtin_amdgcn_fractf(rev_hi * tau) + rev_lo * tau;
  return (f32x2){mag * __builtin_amdgcn_cosf(fr), mag * __builtin_amdgcn_sinf(fr)};
}
#define DPP(v, ctrl) __int_as_float(__builtin_amdgcn_update_dpp(0, __float_as_int(v), ctrl, 0xf, 0xf, false))

__global__ __launch_bounds__(256) void k0_kernel(const char* __restrict__ x, const char* __restrict__ Are, const char* __restrict__ Aim,
                                                 const char* __restrict__ Bre, const char* __restrict__ Bim,
                                                 const char* __restrict__ Cre, const char* __restrict__ Cim,
                                                 const char* __restrict__ logdt, const char* __restrict__ wd,
                                                 const char* __restrict__ wg, const char* __restrict__ wu, char* __restrict__ ws) {
  extern __shared__ __attribute__((aligned(16))) char lds[];
  const int tid = threadIdx.x, lane = tid & 63;
  const int wid = __builtin_amdgcn_readfirstlane(tid >> 6);
  const int r = lane & 15, q = lane >> 4, qq = (lane & 15) >> 2, p = lane & 3;
  const int bid = blockIdx.x;
  if (bid >= 632) {
    const int xcd = bid & 7, part = (bid - 632) >> 3;
    const char* base = (tid < 128 ? wg : wu) + (size_t)(14 * part) * (FF * 2) + (xcd >> 2) * 1792 + (tid & 127) * 16;
    u32x4 acc = {0, 0, 0, 0};
    if ((tid & 127) < 112) {
      u32x4 v[14];
#pragma unroll
      for (int i = 0; i < 14; ++i) v[i] = *(const u32x4*)(base + (size_t)i * (FF * 2));
#pragma unroll
      for (int i = 0; i < 14; ++i) acc ^= v[i];
    }
    asm volatile("" :: "v"(acc));
    return;
  }
  if (bid >= 576) {
    if (bid == 576 && tid < 7) *(unsigned*)(ws + WS_CNT + tid * 256) = 0u;
    const unsigned lbase = (unsigned)(size_t)(LAS char*)lds;
    const int u = (bid - 576) * 4 + wid, s2 = u >> 2, cq = u & 3;
    const char* src = wd + (size_t)(32 * s2) * (DM * 2) + cq * 224;
#pragma unroll
    for (int i = 0; i < 7; ++i) glds16(src + i * 32, (lane >> 1) * (DM * 2) + (lane & 1) * 16, lbase + wid * 7168 + i * 1024);
    WAIT_V(0);
    const char* bp = lds + wid * 7168 + (4 * q + qq) * 32 + p * 8;
#pragma unroll
    for (int i = 0; i < 7; ++i) {
      u32x2 lo = tr_read(bp + i * 1024), hi = tr_read(bp + i * 1024 + 512);
      *(u32x4*)(ws + WS_WD + (size_t)(s2 * 28 + 7 * cq + i) * 1024 + lane * 16) = (u32x4){lo[0], lo[1], hi[0], hi[1]};
    }
    return;
  }
  if (bid >= 448) {
    const int tok0 = (bid - 448) * 16;
    const size_t rowo = (size_t)(tok0 + r) * (DM * 2) + q * 16 + wid * 64;
    u32x4 xr[4];
#pragma unroll
    for (int i = 0; i < 3; ++i) xr[i] = *(const u32x4*)(x + rowo + i * 256);
    xr[3] = (u32x4){0, 0, 0, 0};
    if (wid < 2) xr[3] = *(const u32x4*)(x + rowo + 768);
    float s1 = 0, s2 = 0;
#pragma unroll
    for (int i = 0; i < 4; ++i)
#pragma unroll
      for (int w = 0; w < 4; ++w) {
        const unsigned v = xr[i][w];
        const float lo = bf_lo(v), hi = bf_hi(v);
        s1 += lo + hi;
        s2 = fmaf(lo, lo, fmaf(hi, hi, s2));
      }
    s1 = sum_q(s1); s2 = sum_q(s2);
    if (q == 0) *(f32x2*)(lds + (wid * 16 + r) * 8) = (f32x2){s1, s2};
    __syncthreads();
    float t1 = 0, t2 = 0;
#pragma unroll
    for (int w = 0; w < 4; ++w) { const f32x2 v = *(const f32x2*)(lds + (w * 16 + r) * 8); t1 += v.x; t2 += v.y; }
    const float mean = t1 * (1.0f / DM);
    const float var = fmaxf(t2 * (1.0f / DM) - mean * mean, 0.0f);
    const float mb = rbf(mean);
    const float rs = 1.0f / rbf(sqrtf(rbf(rbf(var) + 1e-5f)));
    char* XT = lds + 1024 + wid * 4096;
#pragma unroll
    for (int i = 0; i < 4; ++i) {
      if (i < 3 || wid < 2) {
        u32x4 o;
#pragma unroll
        for (int w = 0; w < 4; ++w) {
          const unsigned v = xr[i][w];
          const unsigned d = pk_bf16o(bf_lo(v) - mb, bf_hi(v) - mb);
          o[w] = pk_bf16o(bf_lo(d) * rs, bf_hi(d) * rs);
        }
        *(u32x4*)(ws + WS_XN + rowo + i * 256) = o;
        *(u32x4*)(XT + i * 1024 + lane * 16) = o;
      }
    }
    __builtin_amdgcn_wave_barrier();
#pragma unroll
    for (int i = 0; i < 4; ++i) {
      if (i < 3 || wid < 2) {
#pragma unroll
        for (int cb = 0; cb < 2; ++cb) {
          u32x2 v = tr_read(XT + i * 1024 + ((2 * cb + (p >> 1)) * 16 + 4 * q + qq) * 16 + 8 * (p & 1));
          u32x2 o = {pk_f16(bf_lo(v[0]), bf_hi(v[0])), pk_f16(bf_lo(v[1]), bf_hi(v[1]))};
          *(u32x2*)(ws + WS_XT + ((size_t)(32 * (wid + 4 * i) + 16 * cb + r) * SEQ + tok0 + 4 * q) * 2) = o;
        }
      }
    }
    return;
  }
  const int d = bid;
  ST(2, 0);
  const int n = d * NS + tid;
  const float a_re = bf1(Are, n), a_im = bf1(Aim, n), b_re = bf1(Bre, n), b_im = bf1(Bim, n), cr = bf1(Cre, n), ci = bf1(Cim, n);
  const float ldt = bf1(logdt, d);
  u32x4 kidx;
#pragma unroll
  for (int w = 0; w < 4; ++w) {
    unsigned e[2];
#pragma unroll
    for (int h = 0; h < 2; ++h) {
      const int lag = 2047 - 8 * tid - 2 * w - h;
      const int lb = (int)rbf((float)lag);
      const int seg = (31 - __builtin_clz(lb | 128)) - 7;
      const int o = lb & ~(seg ? (128 << seg) : 0);
      const int row = (seg ? 8 + 8 * seg : 0) + ((o & 7) >> seg) + ((o >> 7) << 3 >> seg);
      e[h] = (row * 16 + ((lb >> 3) & 15)) * 2;
    }
    kidx[w] = e[0] | (e[1] << 16);
  }
  asm volatile("" : "+v"(kidx));
  {
    const float dt = rbf(__expf(ldt));
    const float re = dt * a_re, im = dt * a_im, br = dt * b_re, bi = dt * b_im;
    const float CBr = cr * br - ci * bi, CBi = cr * bi + ci * br;
    const float re_l2 = re * 1.44269504088896340736f;
    const float rev_hi = im * 0.15915494309189535f;
    const float rev_lo = fmaf(im, 0.15915494309189535f, -rev_hi) + im * 6.4206e-9f;
    const float sig = fmaxf(1.0f, __builtin_amdgcn_exp2f(re_l2 * 120.0f));
    const float gmx = fmaxf(1.0f, __builtin_amdgcn_exp2f(re_l2 * 2048.0f));
    const float cb1 = (fabsf(CBr) + fabsf(CBi)) * gmx;
    float mx = cb1 * sig, sm = cb1;
    mx = fmaxf(mx, DPP(mx, 0x111)); sm += DPP(sm, 0x111);
    mx = fmaxf(mx, DPP(mx, 0x112)); sm += DPP(sm, 0x112);
    mx = fmaxf(mx, DPP(mx, 0x114)); sm += DPP(sm, 0x114);
    mx = fmaxf(mx, DPP(mx, 0x118)); sm += DPP(sm, 0x118);
    mx = fmaxf(mx, DPP(mx, 0x142)); sm += DPP(sm, 0x142);
    mx = fmaxf(mx, DPP(mx, 0x143)); sm += DPP(sm, 0x143);
    if (lane == 63) *(float*)(lds + KG_MISC + wid * 4) = sm;
    mx = __int_as_float(__builtin_amdgcn_readlane(__float_as_int(mx), 63));
    const int ew = __builtin_amdgcn_frexp_expf(mx);
    const float scw = __builtin_amdgcn_ldexpf(sig, 15 - ew);
    const float unsc = __builtin_amdgcn_ldexpf(1.0f, ew - 30);
    const f32x2 z1 = cexp_tau(re_l2, rev_hi, rev_lo, 1.0f);
    const f32x2 z8 = cexp_tau(re_l2, rev_hi, rev_lo, 8.0f);
    const f32x2 z128 = cexp_tau(re_l2, rev_hi, rev_lo, 128.0f);
    ST(2, 1);
    char* T = lds + wid * KG_TW;
    char* Tw = T + lane * 4;
    const char* Tr = T + r * 288 + q * 16;
    auto put = [&](int row, f32x2 g) { *(unsigned*)(Tw + row * 288) = pk_f16(g.x, g.y); };
    {
      const f32x2 z8s = rot(z8);
      f32x2 e = {32768.0f * __builtin_amdgcn_rcpf(sig), 0.0f};
#pragma unroll
      for (int j = 0; j < 16; ++j) {
        put(j, e);
        if (j < 15) e = cmul2(e, z8, z8s);
      }
    }
    __builtin_amdgcn_wave_barrier();
    f16x8 E[4];
#pragma unroll
    for (int s = 0; s < 4; ++s) E[s] = *(const f16x8*)(Tr + s * 64);
    __builtin_amdgcn_wave_barrier();
    const f32x2 y1 = {z1.x, -z1.y}, y128 = {z128.x, -z128.y};
    const f32x2 y1s = rot(y1), y128s = rot(y128);
    const f32x2 H0 = {CBr * scw, -CBi * scw};
    const f32x2 y2 = csq(y1), y4 = csq(y2), y256 = csq(y128), y512 = csq(y256), y1024 = csq(y512);
    f32x4 kacc[3];
    auto mm = [&](int t) {
      __builtin_amdgcn_wave_barrier();
      f32x4 a = {0, 0, 0, 0};
#pragma unroll
      for (int s = 0; s < 4; ++s) {
        f16x8 g = *(const f16x8*)(Tr + s * 64);
        a = __builtin_amdgcn_mfma_f32_16x16x32_f16(E[s], g, a, 0, 0, 0);
      }
      kacc[t] = a;
      __builtin_amdgcn_wave_barrier();
    };
    {
      f32x2 g = H0;
#pragma unroll
      for (int i = 0; i < 8; ++i) {
        put(i, g); put(8 + i, cmul2(g, y128, y128s));
        if (i < 7) g = cmul2(g, y1, y1s);
      }
    }
    mm(0);
    {
      const f32x2 y2s = rot(y2);
      f32x2 g = cmul2(H0, y256, rot(y256));
#pragma unroll
      for (int i = 0; i < 4; ++i) {
        put(i, g); put(4 + i, cmul2(g, y128, y128s));
        if (i < 3) g = cmul2(g, y2, y2s);
      }
      f32x2 ga = cmul2(H0, y512, rot(y512)), gb = cmul2(ga, y4, rot(y4));
#pragma unroll
      for (int i = 0; i < 4; ++i) {
        put(8 + 2 * i, ga); put(9 + 2 * i, gb);
        if (i < 3) { ga = cmul2(ga, y128, y128s); gb = cmul2(gb, y128, y128s); }
      }
    }
    mm(1);
    {
      f32x2 g = cmul2(H0, y1024, rot(y1024));
#pragma unroll
      for (int i = 0; i < 9; ++i) {
        put(i, g);
        if (i < 8) g = cmul2(g, y128, y128s);
      }
    }
    mm(2);
#pragma unroll
    for (int t = 0; t < 3; ++t) *(f32x4*)(T + ((16 * t + r) * 16 + 4 * q) * 4) = kacc[t] * unsc;
  }
  ST(2, 2);
  __syncthreads();
  {
    const f32x4 ms = *(const f32x4*)(lds + KG_MISC);
    const int ek = __builtin_amdgcn_frexp_expf((ms[0] + ms[1]) + (ms[2] + ms[3]));
    if (tid == 255) *(float*)(ws + WS_KS + d * 4) = __builtin_amdgcn_ldexpf(1.0f, ek - 15);
    if (tid < 192) {
      const float ksc = __builtin_amdgcn_ldexpf(1.0f, 15 - ek);
      const char* kp = lds + tid * 16;
      f32x4 v = ((*(const f32x4*)kp + *(const f32x4*)(kp + KG_TW)) + (*(const f32x4*)(kp + 2 * KG_TW) + *(const f32x4*)(kp + 3 * KG_TW))) * ksc;
      *(u32x2*)(lds + KG_KC + tid * 8) = (u32x2){pk_f16(v[0], v[1]), pk_f16(v[2], v[3])};
    }
  }
  __syncthreads();
  {
    const char* kc = lds + KG_KC;
    u32x4 o;
#pragma unroll
    for (int w = 0; w < 4; ++w) {
      const unsigned iv = kidx[w];
      f16x2 h;
      h.x = *(const _Float16*)(kc + (iv & 0xffffu));
      h.y = *(const _Float16*)(kc + (iv >> 16));
      o[w] = __builtin_bit_cast(unsigned, h);
    }
    *(u32x4*)(ws + WS_KR + (size_t)d * 4096 + tid * 16) = o;
  }
  ST(2, 3);
}

#define K1_SLOT 49152
#define K1_LDS (3 * K1_SLOT)

__global__ __launch_bounds__(768, 3) void k1_kernel(const char* __restrict__ wg, const char* __restrict__ wu, char* __restrict__ ws) {
  extern __shared__ __attribute__((aligned(16))) char lds[];
  const int tid = threadIdx.x, lane = tid & 63;
  const int wid = __builtin_amdgcn_readfirstlane(tid >> 6);
  const int bid = blockIdx.x;
  const unsigned lbase = (unsigned)(size_t)(LAS char*)lds;
  const int r = lane & 15, q = lane >> 4, qq = (lane & 15) >> 2, p = lane & 3;
  if (bid >= 224) {
    const char* base = ws + WS_WD + (size_t)((bid - 224) >> 3) * 401408 + tid * 16;
    u32x4 acc = {0, 0, 0, 0};
    __builtin_amdgcn_s_sleep(64);
#pragma unroll 1
    for (int it = 0; it < 4; ++it) {
      u32x4 v[8];
#pragma unroll
      for (int i = 0; i < 8; ++i) v[i] = *(const u32x4*)(base + (it * 8 + i) * 12288);
#pragma unroll
      for (int i = 0; i < 8; ++i) acc ^= v[i];
    }
    if (tid < 512) acc ^= *(const u32x4*)(base + 32 * 12288);
    asm volatile("" :: "v"(acc));
    return;
  }
  const int xcd = bid & 7, idx = bid >> 3;
  const int mt = 4 * (xcd & 3) + (idx & 3);
  const int jt = 7 * (xcd >> 2) + (idx >> 2);
  const int m = (wid >> 2) & 1, n = wid & 3;
  ST(0, 0);
  if (wid >= 8) {
    const int pw = wid - 8;
    const unsigned aoff = (lane >> 3) * (DM * 2) + (((lane & 7) ^ (lane >> 3)) * 16);
    const char* xsrc = ws + WS_XN + (size_t)(mt * 128 + pw * 16) * (DM * 2);
    const unsigned boff = (8 * pw + q) * (FF * 2) + ((((r >> 1) ^ ((4 * (pw & 1) + q) & 7)) * 2 + (r & 1)) * 16);
    const char* gsrc = wg + jt * 256;
    const char* usrc = wu + jt * 256;
    auto dma_chunk = [&](int c, int slot) {
      const unsigned dst = lbase + slot * K1_SLOT + pw * 1024;
      const size_t wo = (size_t)c * (64 * FF * 2);
#pragma unroll
      for (int h = 0; h < 2; ++h) {
        glds16(xsrc + c * 128 + h * 64 * DM * 2, aoff, dst + pw * 1024 + h * 8192);
        glds16(xsrc + c * 128 + h * 64 * DM * 2 + 8 * DM * 2, aoff, dst + pw * 1024 + h * 8192 + 1024);
        glds16(gsrc + wo + h * 4 * FF * 2, boff, dst + 16384 + h * 4096);
        glds16(usrc + wo + h * 4 * FF * 2, boff, dst + 32768 + h * 4096);
        glds16(gsrc + wo + (32 + h * 4) * FF * 2, boff, dst + 16384 + 8192 + h * 4096);
        glds16(usrc + wo + (32 + h * 4) * FF * 2, boff, dst + 32768 + 8192 + h * 4096);
      }
    };
    dma_chunk(0, 0); dma_chunk(1, 1);
    WAIT_V(12);
    BAR();
    dma_chunk(2, 2);
    int slot = 0;
#pragma unroll 1
    for (int c = 0; c < 6; ++c) {
      if (c < 5) WAIT_V(12); else WAIT_V(0);
      BAR();
      if (c < 4) dma_chunk(c + 3, slot);
      slot = slot == 2 ? 0 : slot + 1;
    }
    return;
  }
  f32x4 acc[4][4];
#pragma unroll
  for (int i = 0; i < 4; ++i)
#pragma unroll
    for (int j = 0; j < 4; ++j) acc[i][j] = (f32x4){0, 0, 0, 0};
  const int x7 = (4 * q + qq) & 7;
  const int brd0 = 16384 + (4 * q + qq) * 256 + (((2 * n) ^ x7) * 32) + p * 8;
  const int brd1 = brd0 ^ 32;
  const int ard0 = (4 * m) * 2048 + (r >> 3) * 1024 + (r & 7) * 128 + ((q ^ (r & 7)) * 16);
  const int ard1 = ard0 ^ 64;
  u32x4 a[2][4], b[2][4];
  auto rdA = [&](int ks, const char* S, int i) { a[ks][i] = *(const u32x4*)(S + (ks ? ard1 : ard0) + i * 2048); };
  auto rdB = [&](int ks, const char* S, int t) {
    const char* bp = S + ((t & 1) ? brd1 : brd0) + (t >> 1) * 16384 + ks * 8192;
    u32x2 lo = tr_read(bp), hi = tr_read(bp + 4096);
    b[ks][t] = (u32x4){lo[0], lo[1], hi[0], hi[1]};
  };
  auto mm = [&](int ks, const char* S) {
#pragma unroll
    for (int t = 0; t < 4; ++t) {
#pragma unroll
      for (int i = 0; i < 4; ++i) {
        acc[t][i] = __builtin_amdgcn_mfma_f32_16x16x32_bf16(__builtin_bit_cast(bf16x8, b[ks][t]), __builtin_bit_cast(bf16x8, a[ks][i]), acc[t][i], 0, 0, 0);
        const int st = 4 * t + i;
        if (st < 8) { if (st & 1) rdB(1 - ks, S, st >> 1); else rdA(1 - ks, S, st >> 1); }
        __builtin_amdgcn_sched_barrier(0);
      }
    }
    __builtin_amdgcn_s_waitcnt(0xC07F);
  };
  BAR();
  ST(0, 2);
#pragma unroll
  for (int t = 0; t < 4; ++t) { rdA(0, lds, t); rdB(0, lds, t); }
  __builtin_amdgcn_s_waitcnt(0xC07F);
  int slot = 0;
#pragma unroll 1
  for (int c = 0; c < 7; ++c) {
    mm(0, lds + slot * K1_SLOT);
    if (c < 6) {
      BAR();
      ST(0, 3 + c);
      slot = slot == 2 ? 0 : slot + 1;
    }
    mm(1, lds + slot * K1_SLOT);
  }
  ST(0, 9);
#pragma unroll
  for (int i = 0; i < 4; ++i) {
    u32x4 hv;
#pragma unroll
    for (int t = 0; t < 2; ++t) {
      const f32x4 g = acc[t][i], u = acc[t + 2][i];
      unsigned g01 = pk_bf16o(g[0], g[1]), g23 = pk_bf16o(g[2], g[3]);
      unsigned u01 = pk_bf16o(u[0], u[1]), u23 = pk_bf16o(u[2], u[3]);
      hv[2 * t] = pk_bf16(bf_lo(g01) * bf_lo(u01), bf_hi(g01) * bf_hi(u01));
      hv[2 * t + 1] = pk_bf16(bf_lo(g23) * bf_lo(u23), bf_hi(g23) * bf_hi(u23));
    }
    *(u32x4*)(ws + WS_H + ((size_t)(mt * 8 + 4 * m + i) * 56 + 4 * jt + n) * 1024 + lane * 16) = hv;
  }
  ST(0, 10);
}

#define K2_LDS 49152
#define CV_XS 4160
#define CV_KR 32768

__global__ __launch_bounds__(256, 3) void k2_kernel(const char* __restrict__ Dskip, const char* __restrict__ x, char* __restrict__ out, char* ws) {
  extern __shared__ __attribute__((aligned(16))) char lds[];
  const int tid = threadIdx.x, lane = tid & 63;
  const int wid = __builtin_amdgcn_readfirstlane(tid >> 6);
  const int r = lane & 15, q = lane >> 4;
  const int bid = blockIdx.x;
  if (bid >= 448) {
    ST(1, 0);
    const int xcd = bid & 7, idx = (bid - 448) >> 3;
    const int mt = 8 * (xcd & 3) + 4 * (xcd >> 2) + (idx & 3), nt = idx >> 2;
    const int rot0 = 3 * wid + (idx & 3);
    const __amdgpu_buffer_rsrc_t rs = __builtin_amdgcn_make_buffer_rsrc(ws, 0, WS_XT, 0x00020000);
    const int hA = WS_H + ((4 * mt) * 56 + 14 * wid) * 1024;
    const int wB = WS_WD + ((14 * wid) * 28 + 4 * nt) * 1024;
    const unsigned lo16 = lane * 16;
    f32x4 acc[4][4];
    u32x4 a[3][4], b[3][4];
    auto load = [&](int buf, int s) {
      s += rot0; s = s >= 14 ? s - 14 : s;
#pragma unroll
      for (int i = 0; i < 4; ++i) {
        a[buf][i] = __builtin_amdgcn_raw_buffer_load_b128(rs, lo16, hA + (i * 56 + s) * 1024, 0);
        b[buf][i] = __builtin_amdgcn_raw_buffer_load_b128(rs, lo16, wB + (s * 28 + ((i + wid) & 3)) * 1024, 0);
      }
    };
    auto comp = [&](int buf) {
#pragma unroll
      for (int j = 0; j < 4; ++j)
#pragma unroll
        for (int i = 0; i < 4; ++i)
          acc[j][i] = __builtin_amdgcn_mfma_f32_16x16x32_bf16(__builtin_bit_cast(bf16x8, b[buf][j]),
                                                              __builtin_bit_cast(bf16x8, a[buf][i]), acc[j][i], 0, 0, 0);
    };
    load(0, 0); load(1, 1); load(2, 2);
#pragma unroll
    for (int i = 0; i < 4; ++i)
#pragma unroll
      for (int j = 0; j < 4; ++j) acc[i][j] = (f32x4){0, 0, 0, 0};
#pragma unroll 1
    for (int it = 0; it < 9; it += 3) {
      comp(0); load(0, it + 3);
      comp(1); load(1, it + 4);
      comp(2); load(2, it + 5);
    }
    comp(0); load(0, 12);
    comp(1); load(1, 13);
    GAS unsigned* cnt = (GAS unsigned*)(ws + WS_CNT + nt * 256);
    unsigned cv = __hip_atomic_load(cnt, RLX_AGENT);
    comp(2);
    for (unsigned spins = 0; spins < (1u << 22); ++spins) {
      if ((unsigned)__builtin_amdgcn_readfirstlane(cv) == 64u) break;
      __builtin_amdgcn_s_sleep(16);
      cv = __hip_atomic_load(cnt, RLX_AGENT);
    }
    __builtin_amdgcn_sched_barrier(0);
    const __amdgpu_buffer_rsrc_t ry = __builtin_amdgcn_make_buffer_rsrc(ws + WS_YT, 0, 1835008, 0x00020000);
    u32x4 yv4[2];
#pragma unroll
    for (int i = 0; i < 2; ++i) {
      const int c = tid + 256 * i;
      yv4[i] = __builtin_amdgcn_raw_buffer_load_b128(ry, ((64 * nt + (c >> 3)) * SEQ + 64 * mt) * 2 + (c & 7) * 16, 0, 16);
    }
    u32x2 xv[4];
#pragma unroll
    for (int i = 0; i < 4; ++i) xv[i] = *(const u32x2*)(x + ((size_t)(64 * mt + 16 * i + r) * DM + 64 * nt + 16 * wid + 4 * q) * 2);
    __builtin_amdgcn_sched_barrier(0);
    comp(0); comp(1);
    ST(1, 1);
#pragma unroll
    for (int j = 1; j < 4; ++j) {
      char* dp = lds + ((((j + wid) & 3) * 3 + (j - 1)) * 4) * 1024 + lo16;
#pragma unroll
      for (int i = 0; i < 4; ++i) *(f32x4*)(dp + i * 1024) = acc[j][i];
    }
    __syncthreads();
#pragma unroll
    for (int sl = 0; sl < 3; ++sl)
#pragma unroll
      for (int i = 0; i < 4; ++i) acc[0][i] += *(const f32x4*)(lds + ((wid * 3 + sl) * 4 + i) * 1024 + lo16);
    __syncthreads();
#pragma unroll
    for (int i = 0; i < 2; ++i) {
      const int c = tid + 256 * i;
      *(u32x4*)(lds + (c >> 3) * 144 + (c & 7) * 16) = yv4[i];
    }
    __syncthreads();
    {
      const int qq = (lane & 15) >> 2, p = lane & 3;
#pragma unroll
      for (int i = 0; i < 4; ++i) {
        const u32x2 yv = tr_read(lds + (16 * wid + 4 * q + qq) * 144 + (16 * i + 4 * p) * 2);
        const unsigned b01 = pk_bf16o(acc[0][i][0], acc[0][i][1]), b23 = pk_bf16o(acc[0][i][2], acc[0][i][3]);
        const unsigned z01 = pk_bf16o(bf_lo(xv[i][0]) + bf_lo(yv[0]), bf_hi(xv[i][0]) + bf_hi(yv[0]));
        const unsigned z23 = pk_bf16o(bf_lo(xv[i][1]) + bf_lo(yv[1]), bf_hi(xv[i][1]) + bf_hi(yv[1]));
        u32x2 o = {pk_bf16(bf_lo(z01) + bf_lo(b01), bf_hi(z01) + bf_hi(b01)), pk_bf16(bf_lo(z23) + bf_lo(b23), bf_hi(z23) + bf_hi(b23))};
        *(u32x2*)(out + ((size_t)(64 * mt + 16 * i + r) * DM + 64 * nt + 16 * wid + 4 * q) * 2) = o;
      }
    }
    ST(1, 2);
    return;
  }
  const int d = bid;
  ST(1, 0);
  const char* xt = ws + WS_XT + (size_t)d * SEQ * 2;
  const u32x4 xa = *(const u32x4*)(xt + tid * 16);
  const u32x2 xb = *(const u32x2*)(xt + (tid < 255 ? tid * 16 + 16 : 0));
  const u32x4 kr = *(const u32x4*)(ws + WS_KR + (size_t)d * 4096 + tid * 16);
  const float kun = *(const float*)(ws + WS_KS + d * 4);
  const float dsk = bf1(Dskip, d);
  if (tid >= 224) *(u32x4*)(lds + CV_KR + 4096 + (tid - 224) * 16) = (u32x4){0, 0, 0, 0};
  {
    char* xc = lds + tid * 16 + 32;
    const unsigned s0 = __builtin_amdgcn_alignbyte(xa[1], xa[0], 2), s1 = __builtin_amdgcn_alignbyte(xa[2], xa[1], 2),
                   s2 = __builtin_amdgcn_alignbyte(xa[3], xa[2], 2), s3 = __builtin_amdgcn_alignbyte(xb[0], xa[3], 2),
                   s4 = __builtin_amdgcn_alignbyte(xb[1], xb[0], 2);
    *(u32x4*)(xc) = xa;
    *(u32x4*)(xc + CV_XS) = (u32x4){s0, s1, s2, s3};
    *(u32x4*)(xc + 2 * CV_XS) = (u32x4){xa[1], xa[2], xa[3], xb[0]};
    *(u32x4*)(xc + 3 * CV_XS) = (u32x4){s1, s2, s3, s4};
    if (tid == 0) {
      u32x4 z = {0, 0, 0, 0};
      *(u32x4*)(lds) = z; *(u32x4*)(lds + 16) = z;
      *(u32x4*)(lds + CV_XS) = z; *(u32x4*)(lds + CV_XS + 16) = (u32x4){0, 0, 0, xa[0] << 16};
      *(u32x4*)(lds + 2 * CV_XS) = z; *(u32x4*)(lds + 2 * CV_XS + 16) = (u32x4){0, 0, 0, xa[0]};
      *(u32x4*)(lds + 3 * CV_XS) = z; *(u32x4*)(lds + 3 * CV_XS + 16) = (u32x4){0, 0, xa[0] << 16, s0};
    }
    *(u32x4*)(lds + CV_KR + tid * 16) = kr;
  }
  __syncthreads();
  ST(1, 4);
  f32x4 acc[8];
  {
    const int e0 = 8 * q + r + 1;
    const char* xp = lds + (e0 & 3) * CV_XS + (e0 & ~3) * 2 + wid * 128;
    const char* kp = lds + CV_KR + (2032 + 64 * wid - 16 * r + 8 * q) * 2;
#pragma unroll
    for (int rr = 0; rr < 2; ++rr) {
      f16x8 G[8];
#pragma unroll
      for (int dl = 0; dl < 8; ++dl) G[dl] = *(const f16x8*)(kp + rr * 64 - dl * 512);
#pragma unroll
      for (int i = 0; i < 8; ++i) {
        const char* fp = xp + (rr + 8 * i) * 64;
        u32x2 f0 = *(const u32x2*)fp, f1 = *(const u32x2*)(fp + 8);
        f16x8 Fr = __builtin_bit_cast(f16x8, (u32x4){f0[0], f0[1], f1[0], f1[1]});
#pragma unroll
        for (int nn = i; nn < 8; ++nn)
          acc[nn] = __builtin_amdgcn_mfma_f32_16x16x32_f16(Fr, G[nn - i], (rr == 0 && i == 0) ? (f32x4){0, 0, 0, 0} : acc[nn], 0, 0, 0);
      }
    }
  }
  ST(1, 5);
  u32x2 xo[2];
#pragma unroll
  for (int i = 0; i < 2; ++i) xo[i] = *(const u32x2*)(lds + (256 * (2 * wid + i) + 16 * r + 4 * q + 16) * 2);
  __syncthreads();
#pragma unroll
  for (int j = 0; j < 8; ++j) *(f32x4*)(lds + (j * 4 + wid) * 1024 + lane * 16) = acc[j];
  __syncthreads();
  u32x2 yv[2];
#pragma unroll
  for (int i = 0; i < 2; ++i) {
    const char* rp = lds + (2 * wid + i) * 4096 + lane * 16;
    f32x4 o = ((*(const f32x4*)rp + *(const f32x4*)(rp + 1024)) + (*(const f32x4*)(rp + 2048) + *(const f32x4*)(rp + 3072))) * kun;
    const u32x2 xoi = xo[i];
    f16x4 xv = __builtin_bit_cast(f16x4, xoi);
    const unsigned c01 = pk_bf16o(o[0], o[1]), c23 = pk_bf16o(o[2], o[3]);
    const unsigned s01 = pk_bf16o((float)xv[0] * dsk, (float)xv[1] * dsk), s23 = pk_bf16o((float)xv[2] * dsk, (float)xv[3] * dsk);
    yv[i] = (u32x2){pk_bf16(bf_lo(c01) + bf_lo(s01), bf_hi(c01) + bf_hi(s01)), pk_bf16(bf_lo(c23) + bf_lo(s23), bf_hi(c23) + bf_hi(s23))};
  }
  {
    auto e0 = __builtin_amdgcn_permlane16_swap(yv[0][0], yv[1][0], false, false);
    auto e1 = __builtin_amdgcn_permlane16_swap(yv[0][1], yv[1][1], false, false);
    const __amdgpu_buffer_rsrc_t ry = __builtin_amdgcn_make_buffer_rsrc(ws + WS_YT, 0, 1835008, 0x00020000);
    __builtin_amdgcn_raw_buffer_store_b128((u32x4){e0[0], e1[0], e0[1], e1[1]}, ry,
                                           (d * SEQ + 256 * (2 * wid + (q & 1)) + 16 * r + 8 * (q >> 1)) * 2, 0, 16);
  }
  asm volatile("s_waitcnt vmcnt(0)" ::: "memory");
  __syncthreads();
  if (tid == 0) __hip_atomic_fetch_add((GAS unsigned*)(ws + WS_CNT + (d >> 6) * 256), 1u, RLX_AGENT);
  ST(1, 6);
}

extern "C" void kernel_launch(void* const* d_in, const int* in_sizes, int n_in, void* d_out, int out_size, void* d_ws,
                              size_t ws_size, hipStream_t stream) {
  const char* x = (const char*)d_in[0];
  char* ws = (char*)d_ws;
  (void)hipFuncSetAttribute((const void*)k1_kernel, hipFuncAttributeMaxDynamicSharedMemorySize, K1_LDS);
  (void)hipFuncSetAttribute((const void*)k2_kernel, hipFuncAttributeMaxDynamicSharedMemorySize, K2_LDS);
  k0_kernel<<<448 + 128 + 56 + 256, 256, K0_LDS, stream>>>(x, (const char*)d_in[1], (const char*)d_in[2], (const char*)d_in[3],
                                                     (const char*)d_in[4], (const char*)d_in[5], (const char*)d_in[6],
                                                     (const char*)d_in[7], (const char*)d_in[12], (const char*)d_in[10], (const char*)d_in[11], ws);
  k1_kernel<<<256, 768, K1_LDS, stream>>>((const char*)d_in[10], (const char*)d_in[11], ws);
  k2_kernel<<<448 + 224, 256, K2_LDS, stream>>>((const char*)d_in[8], x, (char*)d_out, ws);
}
```

```cpp
#include <hip/hip_runtime.h>
#include <hip/hip_bf16.h>
#include <stdint.h>

typedef __attribute__((ext_vector_type(8))) __bf16 bf16x8;
typedef __attribute__((ext_vector_type(2))) __bf16 bf16x2;
typedef __attribute__((ext_vector_type(8))) _Float16 f16x8;
typedef __attribute__((ext_vector_type(4))) _Float16 f16x4;
typedef __attribute__((ext_vector_type(2))) _Float16 f16x2;
typedef __attribute__((ext_vector_type(4))) float f32x4;
typedef __attribute__((ext_vector_type(2))) float f32x2;
typedef __attribute__((ext_vector_type(4))) unsigned u32x4;
typedef __attribute__((ext_vector_type(2))) unsigned u32x2;
typedef __attribute__((ext_vector_type(4))) short s16x4;
#define LAS __attribute__((address_space(3)))
#define GAS __attribute__((address_space(1)))
#define RLX_AGENT __ATOMIC_RELAXED, __HIP_MEMORY_SCOPE_AGENT

#define SEQ 2048
#define DM 448
#define NS 256
#define FF 1792

#define WS_H 0u
#define WS_WD 7340032u
#define WS_XT (WS_WD + 1605632u)
#define WS_YT (WS_XT + 1835008u)
#define WS_KR (WS_YT + 1835008u)
#define WS_KS (WS_KR + 1835008u)
#define WS_CNT (WS_KS + 2048u)

__device__ const unsigned short c_kidx[2048] __attribute__((aligned(16))) = {1280,1280,1280,1280,1278,1278,1278,1278,1278,1278,1278,1276,1276,1276,1276,1276,1276,1276,1276,1276,1274,1274,1274,1274,1274,1274,1274,1272,1272,1272,1272,1272,1272,1272,1272,1272,1270,1270,1270,1270,1270,1270,1270,1268,1268,1268,1268,1268,1268,1268,1268,1268,1266,1266,1266,1266,1266,1266,1266,1264,1264,1264,1264,1264,1264,1264,1264,1264,1262,1262,1262,1262,1262,1262,1262,1260,1260,1260,1260,1260,1260,1260,1260,1260,1258,1258,1258,1258,1258,1258,1258,1256,1256,1256,1256,1256,1256,1256,1256,1256,1254,1254,1254,1254,1254,1254,1254,1252,1252,1252,1252,1252,1252,1252,1252,1252,1250,1250,1250,1250,1250,1250,1250,1248,1248,1248,1248,1248,1248,1248,1248,1248,1246,1246,1246,1246,1246,1246,1246,1244,1244,1244,1244,1244,1244,1244,1244,1244,1242,1242,1242,1242,1242,1242,1242,1240,1240,1240,1240,1240,1240,1240,1240,1240,1238,1238,1238,1238,1238,1238,1238,1236,1236,1236,1236,1236,1236,1236,1236,1236,1234,1234,1234,1234,1234,1234,1234,1232,1232,1232,1232,1232,1232,1232,1232,1232,1230,1230,1230,1230,1230,1230,1230,1228,1228,1228,1228,1228,1228,1228,1228,1228,1226,1226,1226,1226,1226,1226,1226,1224,1224,1224,1224,1224,1224,1224,1224,1224,1222,1222,1222,1222,1222,1222,1222,1220,1220,1220,1220,1220,1220,1220,1220,1220,1218,1218,1218,1218,1218,1218,1218,1216,1216,1216,1216,1216,1216,1216,1216,1216,1214,1214,1214,1214,1214,1214,1214,1212,1212,1212,1212,1212,1212,1212,1212,1212,1210,1210,1210,1210,1210,1210,1210,1208,1208,1208,1208,1208,1208,1208,1208,1208,1206,1206,1206,1206,1206,1206,1206,1204,1204,1204,1204,1204,1204,1204,1204,1204,1202,1202,1202,1202,1202,1202,1202,1200,1200,1200,1200,1200,1200,1200,1200,1200,1198,1198,1198,1198,1198,1198,1198,1196,1196,1196,1196,1196,1196,1196,1196,1196,1194,1194,1194,1194,1194,1194,1194,1192,1192,1192,1192,1192,1192,1192,1192,1192,1190,1190,1190,1190,1190,1190,1190,1188,1188,1188,1188,1188,1188,1188,1188,1188,1186,1186,1186,1186,1186,1186,1186,1184,1184,1184,1184,1184,1184,1184,1184,1184,1182,1182,1182,1182,1182,1182,1182,1180,1180,1180,1180,1180,1180,1180,1180,1180,1178,1178,1178,1178,1178,1178,1178,1176,1176,1176,1176,1176,1176,1176,1176,1176,1174,1174,1174,1174,1174,1174,1174,1172,1172,1172,1172,1172,1172,1172,1172,1172,1170,1170,1170,1170,1170,1170,1170,1168,1168,1168,1168,1168,1168,1168,1168,1168,1166,1166,1166,1166,1166,1166,1166,1164,1164,1164,1164,1164,1164,1164,1164,1164,1162,1162,1162,1162,1162,1162,1162,1160,1160,1160,1160,1160,1160,1160,1160,1160,1158,1158,1158,1158,1158,1158,1158,1156,1156,1156,1156,1156,1156,1156,1156,1156,1154,1154,1154,1154,1154,1154,1154,1152,1152,1152,1152,1152,1152,1152,1152,1152,1150,1150,1150,1150,1150,1150,1150,1148,1148,1148,1148,1148,1148,1148,1148,1148,1146,1146,1146,1146,1146,1146,1146,1144,1144,1144,1144,1144,1144,1144,1144,1144,1142,1142,1142,1142,1142,1142,1142,1140,1140,1140,1140,1140,1140,1140,1140,1140,1138,1138,1138,1138,1138,1138,1138,1136,1136,1136,1136,1136,1136,1136,1136,1136,1134,1134,1134,1134,1134,1134,1134,1132,1132,1132,1132,1132,1132,1132,1132,1132,1130,1130,1130,1130,1130,1130,1130,1128,1128,1128,1128,1128,1128,1128,1128,1128,1126,1126,1126,1126,1126,1126,1126,1124,1124,1124,1124,1124,1124,1124,1124,1124,1122,1122,1122,1122,1122,1122,1122,1120,1120,1120,1120,1120,1120,1120,1120,1120,1118,1118,1118,1118,1118,1118,1118,1116,1116,1116,1116,1116,1116,1116,1116,1116,1114,1114,1114,1114,1114,1114,1114,1112,1112,1112,1112,1112,1112,1112,1112,1112,1110,1110,1110,1110,1110,1110,1110,1108,1108,1108,1108,1108,1108,1108,1108,1108,1106,1106,1106,1106,1106,1106,1106,1104,1104,1104,1104,1104,1104,1104,1104,1104,1102,1102,1102,1102,1102,1102,1102,1100,1100,1100,1100,1100,1100,1100,1100,1100,1098,1098,1098,1098,1098,1098,1098,1096,1096,1096,1096,1096,1096,1096,1096,1096,1094,1094,1094,1094,1094,1094,1094,1092,1092,1092,1092,1092,1092,1092,1092,1092,1090,1090,1090,1090,1090,1090,1090,1088,1088,1088,1088,1088,1088,1088,1088,1088,1086,1086,1086,1086,1086,1086,1086,1084,1084,1084,1084,1084,1084,1084,1084,1084,1082,1082,1082,1082,1082,1082,1082,1080,1080,1080,1080,1080,1080,1080,1080,1080,1078,1078,1078,1078,1078,1078,1078,1076,1076,1076,1076,1076,1076,1076,1076,1076,1074,1074,1074,1074,1074,1074,1074,1072,1072,1072,1072,1072,1072,1072,1072,1072,1070,1070,1070,1070,1070,1070,1070,1068,1068,1068,1068,1068,1068,1068,1068,1068,1066,1066,1066,1066,1066,1066,1066,1064,1064,1064,1064,1064,1064,1064,1064,1064,1062,1062,1062,1062,1062,1062,1062,1060,1060,1060,1060,1060,1060,1060,1060,1060,1058,1058,1058,1058,1058,1058,1058,1056,1056,1056,1056,1056,1056,1056,1056,1056,1054,1054,1054,1054,1054,1054,1054,1052,1052,1052,1052,1052,1052,1052,1052,1052,1050,1050,1050,1050,1050,1050,1050,1048,1048,1048,1048,1048,1048,1048,1048,1048,1046,1046,1046,1046,1046,1046,1046,1044,1044,1044,1044,1044,1044,1044,1044,1044,1042,1042,1042,1042,1042,1042,1042,1040,1040,1040,1040,1040,1040,1040,1040,1040,1038,1038,1038,1038,1038,1038,1038,1036,1036,1036,1036,1036,1036,1036,1036,1036,1034,1034,1034,1034,1034,1034,1034,1032,1032,1032,1032,1032,1032,1032,1032,1032,1030,1030,1030,1030,1030,1030,1030,1028,1028,1028,1028,1028,1028,1028,1028,1028,1026,1026,1026,1026,1026,1026,1026,1024,1024,1024,1024,1024,1024,1024,1022,1022,1022,990,990,990,990,990,1020,1020,1020,988,988,988,988,988,1018,1018,1018,986,986,986,986,986,1016,1016,1016,984,984,984,984,984,1014,1014,1014,982,982,982,982,982,1012,1012,1012,980,980,980,980,980,1010,1010,1010,978,978,978,978,978,1008,1008,1008,976,976,976,976,976,1006,1006,1006,974,974,974,974,974,1004,1004,1004,972,972,972,972,972,1002,1002,1002,970,970,970,970,970,1000,1000,1000,968,968,968,968,968,998,998,998,966,966,966,966,966,996,996,996,964,964,964,964,964,994,994,994,962,962,962,962,962,992,992,992,960,960,960,960,960,958,958,958,926,926,926,926,926,956,956,956,924,924,924,924,924,954,954,954,922,922,922,922,922,952,952,952,920,920,920,920,920,950,950,950,918,918,918,918,918,948,948,948,916,916,916,916,916,946,946,946,914,914,914,914,914,944,944,944,912,912,912,912,912,942,942,942,910,910,910,910,910,940,940,940,908,908,908,908,908,938,938,938,906,906,906,906,906,936,936,936,904,904,904,904,904,934,934,934,902,902,902,902,902,932,932,932,900,900,900,900,900,930,930,930,898,898,898,898,898,928,928,928,896,896,896,896,896,894,894,894,862,862,862,862,862,892,892,892,860,860,860,860,860,890,890,890,858,858,858,858,858,888,888,888,856,856,856,856,856,886,886,886,854,854,854,854,854,884,884,884,852,852,852,852,852,882,882,882,850,850,850,850,850,880,880,880,848,848,848,848,848,878,878,878,846,846,846,846,846,876,876,876,844,844,844,844,844,874,874,874,842,842,842,842,842,872,872,872,840,840,840,840,840,870,870,870,838,838,838,838,838,868,868,868,836,836,836,836,836,866,866,866,834,834,834,834,834,864,864,864,832,832,832,832,832,830,830,830,798,798,798,798,798,828,828,828,796,796,796,796,796,826,826,826,794,794,794,794,794,824,824,824,792,792,792,792,792,822,822,822,790,790,790,790,790,820,820,820,788,788,788,788,788,818,818,818,786,786,786,786,786,816,816,816,784,784,784,784,784,814,814,814,782,782,782,782,782,812,812,812,780,780,780,780,780,810,810,810,778,778,778,778,778,808,808,808,776,776,776,776,776,806,806,806,774,774,774,774,774,804,804,804,772,772,772,772,772,802,802,802,770,770,770,770,770,800,800,800,768,768,768,768,766,734,734,734,702,670,670,670,764,732,732,732,700,668,668,668,762,730,730,730,698,666,666,666,760,728,728,728,696,664,664,664,758,726,726,726,694,662,662,662,756,724,724,724,692,660,660,660,754,722,722,722,690,658,658,658,752,720,720,720,688,656,656,656,750,718,718,718,686,654,654,654,748,716,716,716,684,652,652,652,746,714,714,714,682,650,650,650,744,712,712,712,680,648,648,648,742,710,710,710,678,646,646,646,740,708,708,708,676,644,644,644,738,706,706,706,674,642,642,642,736,704,704,704,672,640,640,640,638,606,606,606,574,542,542,542,636,604,604,604,572,540,540,540,634,602,602,602,570,538,538,538,632,600,600,600,568,536,536,536,630,598,598,598,566,534,534,534,628,596,596,596,564,532,532,532,626,594,594,594,562,530,530,530,624,592,592,592,560,528,528,528,622,590,590,590,558,526,526,526,620,588,588,588,556,524,524,524,618,586,586,586,554,522,522,522,616,584,584,584,552,520,520,520,614,582,582,582,550,518,518,518,612,580,580,580,548,516,516,516,610,578,578,578,546,514,514,514,608,576,576,576,544,512,512,510,478,446,414,382,350,318,286,508,476,444,412,380,348,316,284,506,474,442,410,378,346,314,282,504,472,440,408,376,344,312,280,502,470,438,406,374,342,310,278,500,468,436,404,372,340,308,276,498,466,434,402,370,338,306,274,496,464,432,400,368,336,304,272,494,462,430,398,366,334,302,270,492,460,428,396,364,332,300,268,490,458,426,394,362,330,298,266,488,456,424,392,360,328,296,264,486,454,422,390,358,326,294,262,484,452,420,388,356,324,292,260,482,450,418,386,354,322,290,258,480,448,416,384,352,320,288,256,254,222,190,158,126,94,62,30,252,220,188,156,124,92,60,28,250,218,186,154,122,90,58,26,248,216,184,152,120,88,56,24,246,214,182,150,118,86,54,22,244,212,180,148,116,84,52,20,242,210,178,146,114,82,50,18,240,208,176,144,112,80,48,16,238,206,174,142,110,78,46,14,236,204,172,140,108,76,44,12,234,202,170,138,106,74,42,10,232,200,168,136,104,72,40,8,230,198,166,134,102,70,38,6,228,196,164,132,100,68,36,4,226,194,162,130,98,66,34,2,224,192,160,128,96,64,32,0};

__device__ __forceinline__ unsigned pk_bf16(float a, float b) {
  bf16x2 v = {(__bf16)a, (__bf16)b};
  return __builtin_bit_cast(unsigned, v);
}
__device__ __forceinline__ unsigned pk_bf16o(float a, float b) {
  unsigned r;
  asm("v_cvt_pk_bf16_f32 %0, %1, %2" : "=v"(r) : "v"(a), "v"(b));
  return r;
}
__device__ __forceinline__ unsigned pk_f16(float a, float b) {
  f16x2 v = {(_Float16)a, (_Float16)b};
  return __builtin_bit_cast(unsigned, v);
}
__device__ __forceinline__ float bf_lo(unsigned u) { return __uint_as_float(u << 16); }
__device__ __forceinline__ float bf_hi(unsigned u) { return __uint_as_float(u & 0xffff0000u); }
__device__ __forceinline__ float rbf(float x) { return (float)(__bf16)x; }
__device__ __forceinline__ float bf1(const void* p, int i) {
  return __uint_as_float(((unsigned)((const unsigned short*)p)[i]) << 16);
}
__device__ __forceinline__ u32x2 tr_read(const char* lds) {
  s16x4 v = __builtin_amdgcn_ds_read_tr16_b64_v4i16((LAS s16x4*)lds);
  return __builtin_bit_cast(u32x2, v);
}
#define WAIT_V(n) asm volatile("s_waitcnt vmcnt(" #n ")" ::: "memory")
#define BAR() asm volatile("s_waitcnt lgkmcnt(0)\n\ts_barrier" ::: "memory")
__device__ __forceinline__ void glds16(const char* sbase, unsigned voff, unsigned lds_dst) {
  unsigned keep;
  asm volatile("s_mov_b32 %0, m0\n\ts_mov_b32 m0, %3\n\ts_nop 0\n\tglobal_load_lds_dwordx4 %1, %2\n\ts_mov_b32 m0, %0"
               : "=&s"(keep) : "v"(voff), "s"(sbase), "s"(lds_dst) : "memory");
}
#define ONES_BF16 0x3f803f80u
__device__ __forceinline__ float sum_q(float v) {
  unsigned u = __float_as_uint(v);
  auto a = __builtin_amdgcn_permlane16_swap(u, u, false, false);
  float s = __uint_as_float(a[0]) + __uint_as_float(a[1]);
  u = __float_as_uint(s);
  auto b = __builtin_amdgcn_permlane32_swap(u, u, false, false);
  return __uint_as_float(b[0]) + __uint_as_float(b[1]);
}

#define KG_TW 4608
#define KG_KC (4 * KG_TW)
#define KG_MISC (KG_KC + 1536)

__device__ __forceinline__ f32x2 cmul2(f32x2 a, f32x2 b, f32x2 bs) {
  f32x2 t = a.xx * b;
  return __builtin_elementwise_fma(a.yy, bs, t);
}
__device__ __forceinline__ f32x2 rot(f32x2 b) { return (f32x2){-b.y, b.x}; }
__device__ __forceinline__ f32x2 csq(f32x2 a) {
  float r = fmaf(a.x, a.x, -(a.y * a.y)), i = (a.x + a.x) * a.y;
  asm volatile("" : "+v"(r), "+v"(i));
  return (f32x2){r, i};
}
__device__ __forceinline__ f32x2 cexp_tau(float re_l2, float rev_hi, float rev_lo, float tau) {
  float mag = __builtin_amdgcn_exp2f(re_l2 * tau);
  float fr = __builtin_amdgcn_fractf(rev_hi * tau) + rev_lo * tau;
  return (f32x2){mag * __builtin_amdgcn_cosf(fr), mag * __builtin_amdgcn_sinf(fr)};
}
#define DPP(v, ctrl) __int_as_float(__builtin_amdgcn_update_dpp(0, __float_as_int(v), ctrl, 0xf, 0xf, false))

#define KG_LDS 20480
struct SsmIn { const char *Are, *Aim, *Bre, *Bim, *Cre, *Cim, *logdt; };
__device__ __forceinline__ void kgen(char* lds, const SsmIn& in, char* ws, int d, int tid, int wid) {
  const int lane = tid & 63, r = lane & 15, q = lane >> 4;
  const char *Are = in.Are, *Aim = in.Aim, *Bre = in.Bre, *Bim = in.Bim, *Cre = in.Cre, *Cim = in.Cim, *logdt = in.logdt;
  const int n = d * NS + tid;
  const float a_re = bf1(Are, n), a_im = bf1(Aim, n), b_re = bf1(Bre, n), b_im = bf1(Bim, n), cr = bf1(Cre, n), ci = bf1(Cim, n);
  const float ldt = bf1(logdt, d);
  const u32x4 kidx = *(const u32x4*)((const char*)c_kidx + tid * 16);
  {
    const float dt = rbf(__expf(ldt));
    const float re = dt * a_re, im = dt * a_im, br = dt * b_re, bi = dt * b_im;
    const float CBr = cr * br - ci * bi, CBi = cr * bi + ci * br;
    const float re_l2 = re * 1.44269504088896340736f;
    const float rev_hi = im * 0.15915494309189535f;
    const float rev_lo = fmaf(im, 0.15915494309189535f, -rev_hi) + im * 6.4206e-9f;
    const float sig = fmaxf(1.0f, __builtin_amdgcn_exp2f(re_l2 * 120.0f));
    const float gmx = fmaxf(1.0f, __builtin_amdgcn_exp2f(re_l2 * 2048.0f));
    const float cb1 = (fabsf(CBr) + fabsf(CBi)) * gmx;
    float mx = cb1 * sig;
    mx = fmaxf(mx, DPP(mx, 0x111));
    mx = fmaxf(mx, DPP(mx, 0x112));
    mx = fmaxf(mx, DPP(mx, 0x114));
    mx = fmaxf(mx, DPP(mx, 0x118));
    mx = fmaxf(mx, DPP(mx, 0x142));
    mx = fmaxf(mx, DPP(mx, 0x143));
    if (lane == 63) *(float*)(lds + KG_MISC + wid * 4) = mx * 64.0f;
    mx = __int_as_float(__builtin_amdgcn_readlane(__float_as_int(mx), 63));
    const int ew = __builtin_amdgcn_frexp_expf(mx);
    const float scw = __builtin_amdgcn_ldexpf(sig, 15 - ew);
    const float unsc = __builtin_amdgcn_ldexpf(1.0f, ew - 30);
    const f32x2 z1 = cexp_tau(re_l2, rev_hi, rev_lo, 1.0f);
    const f32x2 z8 = cexp_tau(re_l2, rev_hi, rev_lo, 8.0f);
    const f32x2 z128 = cexp_tau(re_l2, rev_hi, rev_lo, 128.0f);

    char* T = lds + wid * KG_TW;
    char* Tw = T + lane * 4;
    const char* Tr = T + r * 288 + q * 16;
    auto put = [&](int row, f32x2 g) { *(unsigned*)(Tw + row * 288) = pk_f16(g.x, g.y); };
    {
      const f32x2 z16 = csq(z8), z32 = csq(z16), z64 = csq(z32);
      const f32x2 z16s = rot(z16), z32s = rot(z32), z64s = rot(z64);
      const float e0 = 32768.0f * __builtin_amdgcn_rcpf(sig);
      f32x2 e[16];
      e[0] = (f32x2){e0, 0.0f}; e[1] = z8 * e0; e[2] = z16 * e0; e[4] = z32 * e0; e[8] = z64 * e0;
      e[3] = cmul2(e[1], z16, z16s);
#pragma unroll
      for (int j = 1; j < 4; ++j) e[4 + j] = cmul2(e[j], z32, z32s);
#pragma unroll
      for (int j = 1; j < 8; ++j) e[8 + j] = cmul2(e[j], z64, z64s);
#pragma unroll
      for (int j = 0; j < 16; ++j) put(j, e[j]);
    }
    __builtin_amdgcn_wave_barrier();
    f16x8 E[4];
#pragma unroll
    for (int s = 0; s < 4; ++s) E[s] = *(const f16x8*)(Tr + s * 64);
    __builtin_amdgcn_wave_barrier();
    const f32x2 y1 = {z1.x, -z1.y}, y128 = {z128.x, -z128.y};
    const f32x2 y1s = rot(y1), y128s = rot(y128);
    const f32x2 H0 = {CBr * scw, -CBi * scw};
    const f32x2 y2 = csq(y1), y4 = csq(y2), y256 = csq(y128), y512 = csq(y256), y1024 = csq(y512);
    f32x4 kacc[3];
    auto mm = [&](int t) {
      __builtin_amdgcn_wave_barrier();
      f32x4 a = {0, 0, 0, 0};
#pragma unroll
      for (int s = 0; s < 4; ++s) {
        f16x8 g = *(const f16x8*)(Tr + s * 64);
        a = __builtin_amdgcn_mfma_f32_16x16x32_f16(E[s], g, a, 0, 0, 0);
      }
      kacc[t] = a;
      __builtin_amdgcn_wave_barrier();
    };
    const f32x2 y2s = rot(y2), y4s = rot(y4), y256s = rot(y256), y512s = rot(y512), y1024s = rot(y1024);
    {
      f32x2 g[16];
      g[0] = H0; g[1] = cmul2(H0, y1, y1s);
#pragma unroll
      for (int j = 0; j < 2; ++j) g[2 + j] = cmul2(g[j], y2, y2s);
#pragma unroll
      for (int j = 0; j < 4; ++j) g[4 + j] = cmul2(g[j], y4, y4s);
#pragma unroll
      for (int j = 0; j < 8; ++j) g[8 + j] = cmul2(g[j], y128, y128s);
#pragma unroll
      for (int j = 0; j < 16; ++j) put(j, g[j]);
    }
    mm(0);
    {
      f32x2 g[16];
      g[0] = cmul2(H0, y256, y256s); g[1] = cmul2(g[0], y2, y2s);
#pragma unroll
      for (int j = 0; j < 2; ++j) g[2 + j] = cmul2(g[j], y4, y4s);
#pragma unroll
      for (int j = 0; j < 4; ++j) g[4 + j] = cmul2(g[j], y128, y128s);
      g[8] = cmul2(H0, y512, y512s); g[9] = cmul2(g[8], y4, y4s);
#pragma unroll
      for (int j = 0; j < 2; ++j) g[10 + j] = cmul2(g[8 + j], y128, y128s);
#pragma unroll
      for (int j = 0; j < 4; ++j) g[12 + j] = cmul2(g[8 + j], y256, y256s);
#pragma unroll
      for (int j = 0; j < 16; ++j) put(j, g[j]);
    }
    mm(1);
    {
      f32x2 g[9];
      g[0] = cmul2(H0, y1024, y1024s); g[1] = cmul2(g[0], y128, y128s);
#pragma unroll
      for (int j = 0; j < 2; ++j) g[2 + j] = cmul2(g[j], y256, y256s);
#pragma unroll
      for (int j = 0; j < 4; ++j) g[4 + j] = cmul2(g[j], y512, y512s);
      g[8] = cmul2(g[0], y1024, y1024s);
#pragma unroll
      for (int j = 0; j < 9; ++j) put(j, g[j]);
    }
    mm(2);
#pragma unroll
    for (int t = 0; t < 3; ++t) *(f32x4*)(T + ((16 * t + r) * 16 + 4 * q) * 4) = kacc[t] * unsc;
  }

  BAR();
  {
    const f32x4 ms = *(const f32x4*)(lds + KG_MISC);
    const int ek = __builtin_amdgcn_frexp_expf((ms[0] + ms[1]) + (ms[2] + ms[3]));
    if (tid == 255) *(float*)(ws + WS_KS + d * 4) = __builtin_amdgcn_ldexpf(1.0f, ek - 15);
    if (tid < 192) {
      const float ksc = __builtin_amdgcn_ldexpf(1.0f, 15 - ek);
      const char* kp = lds + tid * 16;
      f32x4 v = ((*(const f32x4*)kp + *(const f32x4*)(kp + KG_TW)) + (*(const f32x4*)(kp + 2 * KG_TW) + *(const f32x4*)(kp + 3 * KG_TW))) * ksc;
      *(u32x2*)(lds + KG_KC + tid * 8) = (u32x2){pk_f16(v[0], v[1]), pk_f16(v[2], v[3])};
    }
  }
  BAR();
  {
    const char* kc = lds + KG_KC;
    u32x4 o;
#pragma unroll
    for (int w = 0; w < 4; ++w) {
      const unsigned iv = kidx[w];
      f16x2 h;
      h.x = *(const _Float16*)(kc + (iv & 0xffffu));
      h.y = *(const _Float16*)(kc + (iv >> 16));
      o[w] = __builtin_bit_cast(unsigned, h);
    }
    __builtin_amdgcn_raw_buffer_store_b128(o, __builtin_amdgcn_make_buffer_rsrc(ws, 0, WS_KS, 0x00020000), tid * 16 + WS_KR + d * 4096, 0, 16);
  }
}

#define K1_SLOT 49152
#define K1_LST (3 * K1_SLOT)
#define K1_LCS (K1_LST + 1024)
#define K1_LXT (K1_LCS + 1024)
#define K1_LDS (K1_LXT + 8192)

__global__ __launch_bounds__(768, 3) void k1_kernel(const char* __restrict__ x, const char* __restrict__ wg,
                                                    const char* __restrict__ wu, const char* __restrict__ wd,
                                                    SsmIn in, char* ws) {
  extern __shared__ __attribute__((aligned(16))) char lds[];
  const int tid = threadIdx.x, lane = tid & 63;
  const int wid = __builtin_amdgcn_readfirstlane(tid >> 6);
  const int bid = blockIdx.x;
  const unsigned lbase = (unsigned)(size_t)(LAS char*)lds;
  const int r = lane & 15, q = lane >> 4, qq = (lane & 15) >> 2, p = lane & 3;
  const __amdgpu_buffer_rsrc_t rw = __builtin_amdgcn_make_buffer_rsrc(ws, 0, WS_KS, 0x00020000);
  if (bid >= 224) {
    if (bid == 224 && tid >= 512 && tid < 519) *(unsigned*)(ws + WS_CNT + (tid - 512) * 256) = 0u;
    if (wid < 7) {
      const int u = (bid - 224) * 7 + wid, s2 = u >> 2, cq = u & 3;
      const char* src = wd + (size_t)(32 * s2) * (DM * 2) + cq * 224;
#pragma unroll
      for (int i = 0; i < 7; ++i) glds16(src + i * 32, (lane >> 1) * (DM * 2) + (lane & 1) * 16, lbase + wid * 7168 + i * 1024);
      WAIT_V(0);
      const char* bp = lds + wid * 7168 + (4 * q + qq) * 32 + p * 8;
#pragma unroll
      for (int i = 0; i < 7; ++i) {
        u32x2 lo = tr_read(bp + i * 1024), hi = tr_read(bp + i * 1024 + 512);
        __builtin_amdgcn_raw_buffer_store_b128((u32x4){lo[0], lo[1], hi[0], hi[1]}, rw, lane * 16 + WS_WD + (s2 * 28 + 7 * cq + i) * 1024, 0, 16);
      }
    }
    return;
  }
  const int xcd = bid & 7, idx = bid >> 3;
  const int mt = 4 * (xcd & 3) + (idx & 3);
  const int jt = 7 * (xcd >> 2) + (idx >> 2);
  const int m = wid >> 2, n = wid & 3;
  if (wid >= 8) {
    const int pw = wid - 8;
    const unsigned aoff = (lane >> 3) * (DM * 2) + (((lane & 7) ^ (lane >> 3)) * 16);
    const char* xsrc = x + (size_t)(mt * 128 + pw * 16) * (DM * 2);
    const unsigned boff = (8 * pw + q) * (FF * 2) + ((((r >> 1) ^ ((4 * (pw & 1) + q) & 7)) * 2 + (r & 1)) * 16);
    const char* gsrc = wg + jt * 256;
    const char* usrc = wu + jt * 256;
    auto dma_chunk = [&](int c, int slot) {
      const unsigned dst = lbase + slot * K1_SLOT + pw * 1024;
      const size_t wo = (size_t)c * (64 * FF * 2);
#pragma unroll
      for (int h = 0; h < 2; ++h) {
        glds16(xsrc + c * 128 + h * 64 * DM * 2, aoff, dst + pw * 1024 + h * 8192);
        glds16(xsrc + c * 128 + h * 64 * DM * 2 + 8 * DM * 2, aoff, dst + pw * 1024 + h * 8192 + 1024);
        glds16(gsrc + wo + h * 4 * FF * 2, boff, dst + 16384 + h * 4096);
        glds16(usrc + wo + h * 4 * FF * 2, boff, dst + 32768 + h * 4096);
        glds16(gsrc + wo + (32 + h * 4) * FF * 2, boff, dst + 16384 + 8192 + h * 4096);
        glds16(usrc + wo + (32 + h * 4) * FF * 2, boff, dst + 32768 + 8192 + h * 4096);
      }
    };
    dma_chunk(0, 0); dma_chunk(1, 1);
    BAR(); BAR();
    WAIT_V(12);
    BAR();
    dma_chunk(2, 2);
    int slot = 0;
#pragma unroll 1
    for (int c = 0; c < 6; ++c) {
      if (c < 5) WAIT_V(12); else WAIT_V(0);
      BAR();
      if (c < 4) dma_chunk(c + 3, slot);
      slot = slot == 2 ? 0 : slot + 1;
    }
    BAR();
    return;
  }
  kgen(lds + 2 * K1_SLOT + m * KG_LDS, in, ws, 2 * bid + m, tid & 255, n);
  f32x4 acc[4][4];
#pragma unroll
  for (int i = 0; i < 4; ++i)
#pragma unroll
    for (int j = 0; j < 4; ++j) acc[i][j] = (f32x4){0, 0, 0, 0};
  float s1 = 0.0f, s2 = 0.0f, s1b = 0.0f, s2b = 0.0f, tlo = 0.0f, thi = 0.0f;
  f32x4 csa[2] = {{0, 0, 0, 0}, {0, 0, 0, 0}};
  const u32x4 ones4 = {ONES_BF16, ONES_BF16, ONES_BF16, ONES_BF16};
  u32x4 xsave = {0, 0, 0, 0};
  const int x7 = (4 * q + qq) & 7;
  const int brd0 = 16384 + m * 16384 + (4 * q + qq) * 256 + (((2 * n) ^ x7) * 32) + p * 8;
  const int brd1 = brd0 ^ 32;
  const int bx = m ? -16384 : 16384;
  int ard[4];
#pragma unroll
  for (int i = 0; i < 4; ++i) ard[i] = (4 * m + ((i + n) & 3)) * 2048 + (r >> 3) * 1024 + (r & 7) * 128 + ((q ^ (r & 7)) * 16);
  u32x4 a[2][4], b[2][4];
  auto rdA = [&](int ks, const char* S, int i) { a[ks][i] = *(const u32x4*)(S + (ks ? (ard[i] ^ 64) : ard[i])); };
  auto rdB = [&](int ks, const char* S, int t) {
    const char* bp = S + ((t & 1) ? brd1 : brd0) + ((t >> 1) ? bx : 0) + ks * 8192;
    u32x2 lo = tr_read(bp), hi = tr_read(bp + 4096);
    b[ks][t] = (u32x4){lo[0], lo[1], hi[0], hi[1]};
  };
  auto mm = [&](int ks, const char* S, int dc, int dslot) {
#pragma unroll
    for (int t = 0; t < 4; ++t) {
#pragma unroll
      for (int i = 0; i < 4; ++i) {
        acc[t][i] = __builtin_amdgcn_mfma_f32_16x16x32_bf16(__builtin_bit_cast(bf16x8, b[ks][t]), __builtin_bit_cast(bf16x8, a[ks][i]), acc[t][i], 0, 0, 0);
        const int st = 4 * t + i;
        if (st < 8) { if (st & 1) rdB(1 - ks, S, st >> 1); else rdA(1 - ks, S, st >> 1); }
        if (i == 0) { const unsigned v = a[ks][0][t]; tlo = bf_lo(v); thi = bf_hi(v); asm volatile("" : "+v"(tlo), "+v"(thi)); }
        if (i == 1) { s1 += tlo; s1b += thi; asm volatile("" : "+v"(s1), "+v"(s1b)); }
        if (i == 2) { s2 = fmaf(tlo, tlo, s2); s2b = fmaf(thi, thi, s2b); asm volatile("" : "+v"(s2), "+v"(s2b)); }
        if (i == 3 && t < 2) csa[t] = __builtin_amdgcn_mfma_f32_16x16x32_bf16(__builtin_bit_cast(bf16x8, b[ks][t]), __builtin_bit_cast(bf16x8, ones4), csa[t], 0, 0, 0);
        __builtin_amdgcn_sched_barrier(0);
      }
    }
  };
  BAR();
#pragma unroll
  for (int t = 0; t < 4; ++t) { rdA(0, lds, t); rdB(0, lds, t); }
  __builtin_amdgcn_s_waitcnt(0xC07F);
  int slot = 0;
#pragma unroll 1
  for (int c = 0; c < 7; ++c) {
    if (2 * c == jt) { xsave = a[0][0]; asm volatile("" : "+v"(xsave)); }
    mm(0, lds + slot * K1_SLOT, c < 5 ? c + 2 : -1, slot == 0 ? 2 : slot - 1);
    __builtin_amdgcn_s_waitcnt(0xC07F);
    const int pslot = slot;
    if (c < 6) {
      BAR();
      slot = slot == 2 ? 0 : slot + 1;
    }
    if (2 * c + 1 == jt) { xsave = a[1][0]; asm volatile("" : "+v"(xsave)); }
    mm(1, lds + slot * K1_SLOT, c < 4 ? c + 3 : -1, pslot);
    __builtin_amdgcn_s_waitcnt(0xC07F);
  }
  if (r == 0) {
    *(f32x4*)(lds + K1_LCS + (m * 128 + (2 * n) * 16 + 4 * q) * 4) = csa[0];
    *(f32x4*)(lds + K1_LCS + (m * 128 + (2 * n + 1) * 16 + 4 * q) * 4) = csa[1];
  }
  float mbo, rso;
  {
    const float t1 = sum_q(s1 + s1b), t2 = sum_q(s2 + s2b);
    const float mean = t1 * (1.0f / DM);
    const float var = fmaxf(t2 * (1.0f / DM) - mean * mean, 0.0f);
    mbo = rbf(mean);
    rso = __builtin_amdgcn_rcpf(rbf(__builtin_amdgcn_sqrtf(rbf(rbf(var) + 1e-5f))));
    if (q == 0) {
      *(float*)(lds + K1_LST + ((4 * m + n) * 16 + r) * 4) = mbo * rso;
      *(float*)(lds + K1_LST + 512 + ((4 * m + n) * 16 + r) * 4) = rso;
    }
  }
  char* XT = lds + K1_LXT + wid * 1024;
  {
    u32x4 o;
#pragma unroll
    for (int w = 0; w < 4; ++w) {
      const unsigned v = xsave[w];
      const unsigned d = pk_bf16o(bf_lo(v) - mbo, bf_hi(v) - mbo);
      o[w] = pk_bf16o(bf_lo(d) * rso, bf_hi(d) * rso);
    }
    *(u32x4*)(XT + lane * 16) = o;
  }
  BAR();
  f32x4 cv[4];
  float mrv[4], rsvv[4];
#pragma unroll
  for (int t = 0; t < 4; ++t) {
    cv[t] = *(const f32x4*)(lds + K1_LCS + ((((t >> 1) ^ m) * 128) + (2 * n + (t & 1)) * 16 + 4 * q) * 4);
    const int tile = 4 * m + ((t + n) & 3);
    mrv[t] = *(const float*)(lds + K1_LST + (tile * 16 + r) * 4);
    rsvv[t] = *(const float*)(lds + K1_LST + 512 + (tile * 16 + r) * 4);
  }
  {
    u32x2 o[2];
#pragma unroll
    for (int cb = 0; cb < 2; ++cb) {
      u32x2 v = tr_read(XT + ((2 * cb + (p >> 1)) * 16 + 4 * q + qq) * 16 + 8 * (p & 1));
      o[cb] = (u32x2){pk_f16(bf_lo(v[0]), bf_hi(v[0])), pk_f16(bf_lo(v[1]), bf_hi(v[1]))};
    }
    auto e0 = __builtin_amdgcn_permlane16_swap(o[0][0], o[1][0], false, false);
    auto e1 = __builtin_amdgcn_permlane16_swap(o[0][1], o[1][1], false, false);
    __builtin_amdgcn_raw_buffer_store_b128((u32x4){e0[0], e1[0], e0[1], e1[1]}, rw,
                                           WS_XT + ((32 * jt + 16 * (q & 1) + r) * SEQ + mt * 128 + (4 * m + n) * 16 + 8 * (q >> 1)) * 2, 0, 0);
  }
#pragma unroll
  for (int i = 0; i < 4; ++i) {
    const int tile = 4 * m + ((i + n) & 3);
    float mr = mrv[i], rsv = rsvv[i];
    asm volatile("" : "+v"(mr)); asm volatile("" : "+v"(rsv));
    u32x4 hv;
#pragma unroll
    for (int t = 0; t < 2; ++t) {
      f32x4 g = acc[t][i] * rsv - cv[t] * mr;
      f32x4 u = acc[t + 2][i] * rsv - cv[t + 2] * mr;
      unsigned g01 = pk_bf16o(g[0], g[1]), g23 = pk_bf16o(g[2], g[3]);
      unsigned u01 = pk_bf16o(u[0], u[1]), u23 = pk_bf16o(u[2], u[3]);
      hv[2 * t] = pk_bf16(bf_lo(g01) * bf_lo(u01), bf_hi(g01) * bf_hi(u01));
      hv[2 * t + 1] = pk_bf16(bf_lo(g23) * bf_lo(u23), bf_hi(g23) * bf_hi(u23));
    }
    __builtin_amdgcn_raw_buffer_store_b128(hv, rw, lane * 16 + ((mt * 8 + tile) * 56 + 4 * jt + n) * 1024, 0, 16);
  }
}

#define K2_LDS 49152
#define CV_XS 4160
#define CV_KR 32768

__global__ __launch_bounds__(256, 3) void k2_kernel(const char* __restrict__ Dskip, const char* __restrict__ x, char* __restrict__ out, char* ws) {
  extern __shared__ __attribute__((aligned(16))) char lds[];
  const int tid = threadIdx.x, lane = tid & 63;
  const int wid = __builtin_amdgcn_readfirstlane(tid >> 6);
  const int r = lane & 15, q = lane >> 4;
  const int bid = blockIdx.x;
  if (bid >= 448) {
    const int xcd = bid & 7, idx = (bid - 448) >> 3;
    const int mt = 8 * (xcd & 3) + 4 * (xcd >> 2) + (idx & 3), nt = idx >> 2;
    const int rot0 = 3 * wid + (idx & 3);
    const __amdgpu_buffer_rsrc_t rs = __builtin_amdgcn_make_buffer_rsrc(ws, 0, WS_XT, 0x00020000);
    const int hA = WS_H + ((4 * mt) * 56 + 14 * wid) * 1024;
    const int wB = WS_WD + ((14 * wid) * 28 + 4 * nt) * 1024;
    const unsigned lo16 = lane * 16;
    f32x4 acc[4][4];
    u32x4 a[3][4], b[3][4];
    auto load = [&](int buf, int s) {
      s += rot0; s = s >= 14 ? s - 14 : s;
#pragma unroll
      for (int i = 0; i < 4; ++i) {
        a[buf][i] = __builtin_amdgcn_raw_buffer_load_b128(rs, lo16, hA + (i * 56 + s) * 1024, 0);
        b[buf][i] = __builtin_amdgcn_raw_buffer_load_b128(rs, lo16, wB + (s * 28 + ((i + wid) & 3)) * 1024, 0);
      }
    };
    auto comp = [&](int buf) {
#pragma unroll
      for (int j = 0; j < 4; ++j)
#pragma unroll
        for (int i = 0; i < 4; ++i)
          acc[j][i] = __builtin_amdgcn_mfma_f32_16x16x32_bf16(__builtin_bit_cast(bf16x8, b[buf][j]),
                                                              __builtin_bit_cast(bf16x8, a[buf][i]), acc[j][i], 0, 0, 0);
    };
    load(0, 0); load(1, 1); load(2, 2);
#pragma unroll
    for (int i = 0; i < 4; ++i)
#pragma unroll
      for (int j = 0; j < 4; ++j) acc[i][j] = (f32x4){0, 0, 0, 0};
#pragma unroll 1
    for (int it = 0; it < 9; it += 3) {
      comp(0); load(0, it + 3);
      comp(1); load(1, it + 4);
      comp(2); load(2, it + 5);
    }
    comp(0); load(0, 12);
    comp(1); load(1, 13);
    GAS unsigned* cnt = (GAS unsigned*)(ws + WS_CNT + nt * 256);
    unsigned cv = __hip_atomic_load(cnt, RLX_AGENT);
    comp(2);
    for (unsigned spins = 0; spins < (1u << 22); ++spins) {
      if ((unsigned)__builtin_amdgcn_readfirstlane(cv) == 64u) break;
      __builtin_amdgcn_s_sleep(16);
      cv = __hip_atomic_load(cnt, RLX_AGENT);
    }
    __builtin_amdgcn_sched_barrier(0);
    const __amdgpu_buffer_rsrc_t ry = __builtin_amdgcn_make_buffer_rsrc(ws + WS_YT, 0, 1835008, 0x00020000);
    u32x4 yv4[2];
#pragma unroll
    for (int i = 0; i < 2; ++i) {
      const int c = tid + 256 * i;
      yv4[i] = __builtin_amdgcn_raw_buffer_load_b128(ry, ((64 * nt + (c >> 3)) * SEQ + 64 * mt) * 2 + (c & 7) * 16, 0, 16);
    }
    u32x2 xv[4];
#pragma unroll
    for (int i = 0; i < 4; ++i) xv[i] = *(const u32x2*)(x + ((size_t)(64 * mt + 16 * i + r) * DM + 64 * nt + 16 * wid + 4 * q) * 2);
    __builtin_amdgcn_sched_barrier(0);
    comp(0); comp(1);
#pragma unroll
    for (int j = 1; j < 4; ++j) {
      char* dp = lds + ((((j + wid) & 3) * 3 + (j - 1)) * 4) * 1024 + lo16;
#pragma unroll
      for (int i = 0; i < 4; ++i) *(f32x4*)(dp + i * 1024) = acc[j][i];
    }
    __syncthreads();
#pragma unroll
    for (int sl = 0; sl < 3; ++sl)
#pragma unroll
      for (int i = 0; i < 4; ++i) acc[0][i] += *(const f32x4*)(lds + ((wid * 3 + sl) * 4 + i) * 1024 + lo16);
    __syncthreads();
#pragma unroll
    for (int i = 0; i < 2; ++i) {
      const int c = tid + 256 * i;
      *(u32x4*)(lds + (c >> 3) * 144 + (c & 7) * 16) = yv4[i];
    }
    __syncthreads();
    {
      const int qq = (lane & 15) >> 2, p = lane & 3;
#pragma unroll
      for (int i = 0; i < 4; ++i) {
        const u32x2 yv = tr_read(lds + (16 * wid + 4 * q + qq) * 144 + (16 * i + 4 * p) * 2);
        const unsigned b01 = pk_bf16o(acc[0][i][0], acc[0][i][1]), b23 = pk_bf16o(acc[0][i][2], acc[0][i][3]);
        const unsigned z01 = pk_bf16o(bf_lo(xv[i][0]) + bf_lo(yv[0]), bf_hi(xv[i][0]) + bf_hi(yv[0]));
        const unsigned z23 = pk_bf16o(bf_lo(xv[i][1]) + bf_lo(yv[1]), bf_hi(xv[i][1]) + bf_hi(yv[1]));
        u32x2 o = {pk_bf16(bf_lo(z01) + bf_lo(b01), bf_hi(z01) + bf_hi(b01)), pk_bf16(bf_lo(z23) + bf_lo(b23), bf_hi(z23) + bf_hi(b23))};
        *(u32x2*)(out + ((size_t)(64 * mt + 16 * i + r) * DM + 64 * nt + 16 * wid + 4 * q) * 2) = o;
      }
    }
    return;
  }
  const int d = bid;
  const char* xt = ws + WS_XT + (size_t)d * SEQ * 2;
  const u32x4 xa = *(const u32x4*)(xt + tid * 16);
  const u32x2 xb = *(const u32x2*)(xt + (tid < 255 ? tid * 16 + 16 : 0));
  const u32x4 kr = *(const u32x4*)(ws + WS_KR + (size_t)d * 4096 + tid * 16);
  const float kun = *(const float*)(ws + WS_KS + d * 4);
  const float dsk = bf1(Dskip, d);
  if (tid >= 224) *(u32x4*)(lds + CV_KR + 4096 + (tid - 224) * 16) = (u32x4){0, 0, 0, 0};
  {
    char* xc = lds + tid * 16 + 32;
    const unsigned s0 = __builtin_amdgcn_alignbyte(xa[1], xa[0], 2), s1 = __builtin_amdgcn_alignbyte(xa[2], xa[1], 2),
                   s2 = __builtin_amdgcn_alignbyte(xa[3], xa[2], 2), s3 = __builtin_amdgcn_alignbyte(xb[0], xa[3], 2),
                   s4 = __builtin_amdgcn_alignbyte(xb[1], xb[0], 2);
    *(u32x4*)(xc) = xa;
    *(u32x4*)(xc + CV_XS) = (u32x4){s0, s1, s2, s3};
    *(u32x4*)(xc + 2 * CV_XS) = (u32x4){xa[1], xa[2], xa[3], xb[0]};
    *(u32x4*)(xc + 3 * CV_XS) = (u32x4){s1, s2, s3, s4};
    if (tid == 0) {
      u32x4 z = {0, 0, 0, 0};
      *(u32x4*)(lds) = z; *(u32x4*)(lds + 16) = z;
      *(u32x4*)(lds + CV_XS) = z; *(u32x4*)(lds + CV_XS + 16) = (u32x4){0, 0, 0, xa[0] << 16};
      *(u32x4*)(lds + 2 * CV_XS) = z; *(u32x4*)(lds + 2 * CV_XS + 16) = (u32x4){0, 0, 0, xa[0]};
      *(u32x4*)(lds + 3 * CV_XS) = z; *(u32x4*)(lds + 3 * CV_XS + 16) = (u32x4){0, 0, xa[0] << 16, s0};
    }
    *(u32x4*)(lds + CV_KR + tid * 16) = kr;
  }
  __syncthreads();
  f32x4 acc[8];
  {
    const int e0 = 8 * q + r + 1;
    const char* xp = lds + (e0 & 3) * CV_XS + (e0 & ~3) * 2 + wid * 128;
    const char* kp = lds + CV_KR + (2032 + 64 * wid - 16 * r + 8 * q) * 2;
#pragma unroll
    for (int rr = 0; rr < 2; ++rr) {
      f16x8 G[8];
#pragma unroll
      for (int dl = 0; dl < 8; ++dl) G[dl] = *(const f16x8*)(kp + rr * 64 - dl * 512);
#pragma unroll
      for (int i = 0; i < 8; ++i) {
        const char* fp = xp + (rr + 8 * i) * 64;
        u32x2 f0 = *(const u32x2*)fp, f1 = *(const u32x2*)(fp + 8);
        f16x8 Fr = __builtin_bit_cast(f16x8, (u32x4){f0[0], f0[1], f1[0], f1[1]});
#pragma unroll
        for (int nn = i; nn < 8; ++nn)
          acc[nn] = __builtin_amdgcn_mfma_f32_16x16x32_f16(Fr, G[nn - i], (rr == 0 && i == 0) ? (f32x4){0, 0, 0, 0} : acc[nn], 0, 0, 0);
      }
    }
  }
  u32x2 xo[2];
#pragma unroll
  for (int i = 0; i < 2; ++i) xo[i] = *(const u32x2*)(lds + (256 * (2 * wid + i) + 16 * r + 4 * q + 16) * 2);
  __syncthreads();
#pragma unroll
  for (int j = 0; j < 8; ++j) *(f32x4*)(lds + (j * 4 + wid) * 1024 + lane * 16) = acc[j];
  __syncthreads();
  u32x2 yv[2];
#pragma unroll
  for (int i = 0; i < 2; ++i) {
    const char* rp = lds + (2 * wid + i) * 4096 + lane * 16;
    f32x4 o = ((*(const f32x4*)rp + *(const f32x4*)(rp + 1024)) + (*(const f32x4*)(rp + 2048) + *(const f32x4*)(rp + 3072))) * kun;
    const u32x2 xoi = xo[i];
    f16x4 xv = __builtin_bit_cast(f16x4, xoi);
    const unsigned c01 = pk_bf16o(o[0], o[1]), c23 = pk_bf16o(o[2], o[3]);
    const unsigned s01 = pk_bf16o((float)xv[0] * dsk, (float)xv[1] * dsk), s23 = pk_bf16o((float)xv[2] * dsk, (float)xv[3] * dsk);
    yv[i] = (u32x2){pk_bf16(bf_lo(c01) + bf_lo(s01), bf_hi(c01) + bf_hi(s01)), pk_bf16(bf_lo(c23) + bf_lo(s23), bf_hi(c23) + bf_hi(s23))};
  }
  {
    auto e0 = __builtin_amdgcn_permlane16_swap(yv[0][0], yv[1][0], false, false);
    auto e1 = __builtin_amdgcn_permlane16_swap(yv[0][1], yv[1][1], false, false);
    const __amdgpu_buffer_rsrc_t ry = __builtin_amdgcn_make_buffer_rsrc(ws + WS_YT, 0, 1835008, 0x00020000);
    __builtin_amdgcn_raw_buffer_store_b128((u32x4){e0[0], e1[0], e0[1], e1[1]}, ry,
                                           (d * SEQ + 256 * (2 * wid + (q & 1)) + 16 * r + 8 * (q >> 1)) * 2, 0, 16);
  }
  asm volatile("s_waitcnt vmcnt(0)" ::: "memory");
  __syncthreads();
  if (tid == 0) __hip_atomic_fetch_add((GAS unsigned*)(ws + WS_CNT + (d >> 6) * 256), 1u, RLX_AGENT);
}

extern "C" void kernel_launch(void* const* d_in, const int* in_sizes, int n_in, void* d_out, int out_size, void* d_ws,
                              size_t ws_size, hipStream_t stream) {
  const char* x = (const char*)d_in[0];
  char* ws = (char*)d_ws;
  SsmIn in = {(const char*)d_in[1], (const char*)d_in[2], (const char*)d_in[3], (const char*)d_in[4],
              (const char*)d_in[5], (const char*)d_in[6], (const char*)d_in[7]};
  (void)hipFuncSetAttribute((const void*)k1_kernel, hipFuncAttributeMaxDynamicSharedMemorySize, K1_LDS);
  (void)hipFuncSetAttribute((const void*)k2_kernel, hipFuncAttributeMaxDynamicSharedMemorySize, K2_LDS);
  k1_kernel<<<256, 768, K1_LDS, stream>>>(x, (const char*)d_in[10], (const char*)d_in[11], (const char*)d_in[12], in, ws);
  k2_kernel<<<448 + 224, 256, K2_LDS, stream>>>((const char*)d_in[8], x, (char*)d_out, ws);
}
```
